```python
import torch

def _softmax(x, axis=-1):
    return torch.softmax(x, dim=axis)
def _gelu(x):
    return torch.nn.functional.gelu(x, approximate="tanh")
def _silu(x):
    return torch.nn.functional.silu(x)
def _sigmoid(x):
    return torch.sigmoid(x)
def _layernorm(x, axis=-1, eps=1e-5):
    m = x.mean(dim=axis, keepdim=True)
    v = x.var(dim=axis, keepdim=True, unbiased=False)
    return (x - m) / torch.sqrt(v + eps)
def _scatter_add(tgt, idx, src, axis=0):
    return tgt.clone().index_add_(axis, idx, src)
def _segment_sum(src, seg, n):
    out = torch.zeros((n,) + src.shape[1:], dtype=src.dtype)
    return out.index_add_(0, seg, src)
def _pad(x, pw):
    flat = tuple(v for pair in reversed(pw) for v in pair)
    return torch.nn.functional.pad(x, flat)

kernel_name = 'gated_mix__gqa_block_cached__geglu_ffn__decode'

def setup_inputs():

    _w = (lambda a: (lambda t: t.to(torch.bfloat16) if t.is_floating_point() else t)(torch.as_tensor(a)))
    def _A_setup():
        B, Sq, D = 2, 1, 640
        Hq, Hkv, Sk, Dh = 32, 32, 8192, 128
        assert Hq % Hkv == 0, (Hq, Hkv)
        return {
            "x": _w((lambda *s: torch.randn(*s, dtype=torch.bfloat16) * 0.05)(B, Sq, D)),
            "k_cache": _w((lambda *s: torch.randn(*s, dtype=torch.bfloat16) * 6.4)(B, Hkv, Sk, Dh)),
            "v_cache": _w((lambda *s: torch.randn(*s, dtype=torch.bfloat16) * 0.2)(B, Hkv, Sk, Dh)),
            "wq": _w((lambda *s: torch.randn(*s, dtype=torch.bfloat16) * 6.4)(D, Hq * Dh) * (D ** -0.5)),
            "wo": _w((lambda *s: torch.randn(*s, dtype=torch.bfloat16) * 0.2)(Hq * Dh, D) * ((Hq * Dh) ** -0.5)),
        }
    _A_in = _A_setup()

    def _B_setup():
        B, S, D, F = 2, 1, 640, 1728
        return {
            "x": _w((lambda *s: torch.randn(*s, dtype=torch.bfloat16) * 0.05)(B, S, D)),
            "w_gate": _w((lambda *s: torch.randn(*s, dtype=torch.bfloat16) * 0.05)(D, F)),
            "w_up": _w((lambda *s: torch.randn(*s, dtype=torch.bfloat16) * 0.05)(D, F)),
            "w_down": _w((lambda *s: torch.randn(*s, dtype=torch.bfloat16) * 0.05)(F, D)),
        }
    _B_in = _B_setup()

    return {
        "A_x": _A_in["x"],
        "A_k_cache": _A_in["k_cache"],
        "A_v_cache": _A_in["v_cache"],
        "A_wq": _A_in["wq"],
        "A_wo": _A_in["wo"],
        "B_x": _B_in["x"],
        "B_w_gate": _B_in["w_gate"],
        "B_w_up": _B_in["w_up"],
        "B_w_down": _B_in["w_down"],
        "gate_w": _w((lambda *s: torch.randn(*s, dtype=torch.bfloat16) * 0.05)(640) * 16.0),
    }

def reference(A_x, A_k_cache, A_v_cache, A_wq, A_wo, B_x, B_w_gate, B_w_up, B_w_down, gate_w):
    def _A_ref(x, k_cache, v_cache, wq, wo):
        B, Sq, D = x.shape
        Hq, Dh = 32, 128
        Hkv = k_cache.shape[1]
        rep = Hq // Hkv
        q = (lambda x, *d: x.permute(*d))((x @ wq).reshape(B, Sq, Hq, Dh), 0, 2, 1, 3)
        k = (lambda x, n, axis: torch.repeat_interleave(x, n, dim=axis))(k_cache, rep, axis=1)
        v = (lambda x, n, axis: torch.repeat_interleave(x, n, dim=axis))(v_cache, rep, axis=1)
        scores = torch.einsum("bhqd,bhkd->bhqk", q, k) * (Dh ** -0.5)
        p = _softmax(scores, axis=-1)
        o = torch.einsum("bhqk,bhkd->bhqd", p, v)
        return (lambda x, *d: x.permute(*d))(o, 0, 2, 1, 3).reshape(B, Sq, Hq * Dh) @ wo

    def _B_ref(x, w_gate, w_up, w_down):
        return (_gelu(x @ w_gate) * (x @ w_up)) @ w_down

    x = A_x
    g = _sigmoid((x * gate_w).sum(-1, keepdims=True))
    a = _A_ref(x, A_k_cache, A_v_cache, A_wq, A_wo)
    b = _B_ref(x, B_w_gate, B_w_up, B_w_down)
    return g * a + (1 - g) * b
```

```cpp
#include <hip/hip_runtime.h>
#include <hip/hip_bf16.h>
#include <stdint.h>

typedef unsigned int u32;
using u32x4 = __attribute__((ext_vector_type(4))) unsigned;
using bf16x2 = __attribute__((ext_vector_type(2))) __bf16;

constexpr int B = 2, D = 640, H = 32, SK = 8192, DH = 128, F = 1728;
constexpr int NSPLIT = 8;
constexpr int KPS = SK / NSPLIT;

__device__ __forceinline__ float bf2f(unsigned short b) { return __builtin_bit_cast(float, (unsigned)b << 16); }
__device__ __forceinline__ float lo(unsigned u) { return __builtin_bit_cast(float, u << 16); }
__device__ __forceinline__ float hi(unsigned u) { return __builtin_bit_cast(float, u & 0xffff0000u); }
__device__ __forceinline__ float rbf(float x) { return (float)(__bf16)x; }

constexpr size_t WS_Q = 0;
constexpr size_t WS_HID = WS_Q + 2 * 4096;
constexpr size_t WS_PM = WS_HID + 2 * 1728 + 64;
constexpr size_t WS_PO = WS_PM + 64 * NSPLIT * 2;

__global__ void __launch_bounds__(256) k1(const unsigned short* x, const unsigned short* wq, const unsigned short* wg,
                                          const unsigned short* wu, float* ws) {
  __shared__ float red[2][16][128 + 4];
  __shared__ float xs[2][640];
  int tid = threadIdx.x;
  for (int i = tid; i < 2 * 640; i += 256) xs[i / 640][i % 640] = bf2f(x[i]);
  __syncthreads();
  int c = tid & 15, rg = tid >> 4;
  float a0[8], a1[8];
  for (int k = 0; k < 8; k++) a0[k] = a1[k] = 0.f;
  int blk = blockIdx.x;
  const unsigned short* w; int ld;
  if (blk < 32) { w = wq + blk * 128 + c * 8; ld = 4096; }
  else { int j = blk - 32; if (c < 8) w = wg + j * 64 + c * 8; else w = wu + j * 64 + (c - 8) * 8; ld = F; }
  for (int r = rg; r < 640; r += 16) {
    u32x4 v = *(const u32x4*)(w + (size_t)r * ld);
    float x0 = xs[0][r], x1 = xs[1][r];
    for (int k = 0; k < 4; k++) {
      a0[2 * k] += x0 * lo(v[k]); a0[2 * k + 1] += x0 * hi(v[k]);
      a1[2 * k] += x1 * lo(v[k]); a1[2 * k + 1] += x1 * hi(v[k]);
    }
  }
  for (int k = 0; k < 8; k++) { red[0][rg][c * 8 + k] = a0[k]; red[1][rg][c * 8 + k] = a1[k]; }
  __syncthreads();
  int bb = tid >> 7, col = tid & 127;
  float s = 0.f;
  for (int r = 0; r < 16; r++) s += red[bb][r][col];
  s = rbf(s);
  if (blk < 32) ws[WS_Q + bb * 4096 + blk * 128 + col] = s;
  else {
    __syncthreads();
    red[bb][0][col] = s;
    __syncthreads();
    if (col < 64) {
      float g = red[bb][0][col], u = red[bb][0][col + 64];
      float t = 0.7978845608028654f * (g + 0.044715f * g * g * g);
      float ge = rbf(0.5f * g * (1.f + tanhf(t)));
      ws[WS_HID + bb * 1728 + (blk - 32) * 64 + col] = rbf(ge * u);
    }
  }
}

__global__ void __launch_bounds__(256) k2(const unsigned short* kc, const unsigned short* vc, float* ws) {
  __shared__ float sm[4][132];
  int bh = blockIdx.x / NSPLIT, sp = blockIdx.x % NSPLIT;
  int tid = threadIdx.x, lane = tid & 63, wave = tid >> 6, r = lane >> 4, c = lane & 15;
  float q[8];
  { int b = bh / 32, h = bh % 32; for (int k = 0; k < 8; k++) q[k] = ws[WS_Q + b * 4096 + h * 128 + c * 8 + k] * 0.08838834764831845f * 1.4426950408889634f; }
  const unsigned short* kb = kc + ((size_t)bh * SK + sp * KPS) * DH;
  const unsigned short* vb = vc + ((size_t)bh * SK + sp * KPS) * DH;
  float m = -INFINITY, l = 0.f, acc[8];
  for (int k = 0; k < 8; k++) acc[k] = 0.f;
  for (int t = wave * 4 + r; t < KPS; t += 16) {
    u32x4 kv = *(const u32x4*)(kb + (size_t)t * DH + c * 8);
    u32x4 vv = *(const u32x4*)(vb + (size_t)t * DH + c * 8);
    float s = 0.f;
    for (int k = 0; k < 4; k++) { s += q[2 * k] * lo(kv[k]); s += q[2 * k + 1] * hi(kv[k]); }
    s += __shfl_xor(s, 1); s += __shfl_xor(s, 2); s += __shfl_xor(s, 4); s += __shfl_xor(s, 8);
    float mn = fmaxf(m, s);
    float al = exp2f(m - mn), p = exp2f(s - mn);
    l = l * al + p; m = mn;
    for (int k = 0; k < 4; k++) { acc[2 * k] = acc[2 * k] * al + p * lo(vv[k]); acc[2 * k + 1] = acc[2 * k + 1] * al + p * hi(vv[k]); }
  }
  float M = fmaxf(m, __shfl_xor(m, 16)); M = fmaxf(M, __shfl_xor(M, 32));
  float f = exp2f(m - M);
  l *= f; l += __shfl_xor(l, 16); l += __shfl_xor(l, 32);
  for (int k = 0; k < 8; k++) { acc[k] *= f; acc[k] += __shfl_xor(acc[k], 16); acc[k] += __shfl_xor(acc[k], 32); }
  if (r == 0) { for (int k = 0; k < 8; k++) sm[wave][4 + c * 8 + k] = acc[k]; if (c == 0) { sm[wave][0] = M; sm[wave][1] = l; } }
  __syncthreads();
  if (tid < 128) {
    float MM = fmaxf(fmaxf(sm[0][0], sm[1][0]), fmaxf(sm[2][0], sm[3][0]));
    float L = 0.f, o = 0.f;
    for (int w = 0; w < 4; w++) { float e = exp2f(sm[w][0] - MM); L += e * sm[w][1]; o += e * sm[w][4 + tid]; }
    ws[WS_PO + ((size_t)bh * NSPLIT + sp) * 128 + tid] = o;
    if (tid == 0) { ws[WS_PM + (bh * NSPLIT + sp) * 2] = MM; ws[WS_PM + (bh * NSPLIT + sp) * 2 + 1] = L; }
  }
}

__global__ void __launch_bounds__(256) k3(const unsigned short* x, const unsigned short* gw, const unsigned short* wo, const unsigned short* wd, const float* ws, __hip_bfloat16* out) {
  __shared__ float o[4096];
  __shared__ float hs[1728];
  __shared__ float red[32][64];
  __shared__ float red2[32][64];
  __shared__ float gsh[256];
  int b = blockIdx.x / 10, cb = blockIdx.x % 10, tid = threadIdx.x;
  for (int i = tid; i < 4096; i += 256) {
    int h = i >> 7, d = i & 127, bh = b * 32 + h;
    float M = -INFINITY;
    for (int s = 0; s < NSPLIT; s++) M = fmaxf(M, ws[WS_PM + (bh * NSPLIT + s) * 2]);
    float L = 0.f, oo = 0.f;
    for (int s = 0; s < NSPLIT; s++) { float e = exp2f(ws[WS_PM + (bh * NSPLIT + s) * 2] - M); L += e * ws[WS_PM + (bh * NSPLIT + s) * 2 + 1]; oo += e * ws[WS_PO + ((size_t)bh * NSPLIT + s) * 128 + d]; }
    o[i] = rbf(oo / L);
  }
  for (int i = tid; i < 1728; i += 256) hs[i] = ws[WS_HID + b * 1728 + i];
  float gp = 0.f;
  for (int i = tid; i < 640; i += 256) gp += rbf(bf2f(x[b * 640 + i]) * bf2f(gw[i]));
  gsh[tid] = gp;
  __syncthreads();
  for (int s = 128; s > 0; s >>= 1) { if (tid < s) gsh[tid] += gsh[tid + s]; __syncthreads(); }
  float g = rbf(gsh[0]); g = rbf(1.f / (1.f + expf(-g)));
  int c = tid & 7, rg = tid >> 3;
  float a[8], f[8];
  for (int k = 0; k < 8; k++) a[k] = f[k] = 0.f;
  for (int r = rg; r < 4096; r += 32) {
    u32x4 v = *(const u32x4*)(wo + (size_t)r * 640 + cb * 64 + c * 8);
    float ov = o[r];
    for (int k = 0; k < 4; k++) { a[2 * k] += ov * lo(v[k]); a[2 * k + 1] += ov * hi(v[k]); }
  }
  for (int r = rg; r < 1728; r += 32) {
    u32x4 v = *(const u32x4*)(wd + (size_t)r * 640 + cb * 64 + c * 8);
    float ov = hs[r];
    for (int k = 0; k < 4; k++) { f[2 * k] += ov * lo(v[k]); f[2 * k + 1] += ov * hi(v[k]); }
  }
  for (int k = 0; k < 8; k++) { red[rg][c * 8 + k] = a[k]; red2[rg][c * 8 + k] = f[k]; }
  __syncthreads();
  if (tid < 64) {
    float sa = 0.f, sf = 0.f;
    for (int r = 0; r < 32; r++) { sa += red[r][tid]; sf += red2[r][tid]; }
    sa = rbf(sa); sf = rbf(sf);
    float res = rbf(g * sa) + rbf(rbf(1.f - g) * sf);
    out[b * 640 + cb * 64 + tid] = (__hip_bfloat16)res;
  }
}

extern "C" void kernel_launch(void* const* d_in, const int* in_sizes, int n_in, void* d_out, int out_size, void* d_ws, size_t ws_size, hipStream_t stream) {
  const unsigned short* x = (const unsigned short*)d_in[0];
  const unsigned short* kc = (const unsigned short*)d_in[1];
  const unsigned short* vc = (const unsigned short*)d_in[2];
  const unsigned short* wq = (const unsigned short*)d_in[3];
  const unsigned short* wo = (const unsigned short*)d_in[4];
  const unsigned short* wg = (const unsigned short*)d_in[6];
  const unsigned short* wu = (const unsigned short*)d_in[7];
  const unsigned short* wd = (const unsigned short*)d_in[8];
  const unsigned short* gw = (const unsigned short*)d_in[9];
  float* ws = (float*)d_ws;
  k1<<<32 + 27, 256, 0, stream>>>(x, wq, wg, wu, ws);
  k2<<<64 * NSPLIT, 256, 0, stream>>>(kc, vc, ws);
  k3<<<20, 256, 0, stream>>>(x, gw, wo, wd, ws, (__hip_bfloat16*)d_out);
}
```

```cpp
#include <hip/hip_runtime.h>
#include <hip/hip_bf16.h>
#include <stdint.h>

typedef unsigned u32;
using u32x4 = __attribute__((ext_vector_type(4))) unsigned;
using u32x2 = __attribute__((ext_vector_type(2))) unsigned;
using f32x4 = __attribute__((ext_vector_type(4))) float;
using f32x2 = __attribute__((ext_vector_type(2))) float;
using bf16x2 = __attribute__((ext_vector_type(2))) __bf16;
typedef __amdgpu_buffer_rsrc_t rsrc_t;

#define RING 2
#define TAILAT 4

constexpr int D = 640, H = 32, SK = 8192, DH = 128, F = 1728;
constexpr int NWG = 256, NTHR = 512;
constexpr int AUX_NT = 2, AUX_SC1 = 16;
constexpr int NSLOT = 64;

constexpr unsigned WS_EPOCH = 0;
constexpr unsigned WS_HOP1 = 4096, HOP1_STRIDE = 1152;
constexpr unsigned WS_FFN = WS_HOP1 + 256 * HOP1_STRIDE, FFN_STRIDE = 2048;
constexpr unsigned WS_HOP2 = WS_FFN + 256 * FFN_STRIDE, HOP2_STRIDE = 1280;
constexpr unsigned WS_END = WS_HOP2 + 256 * HOP2_STRIDE;

constexpr int L_XS = 0;
constexpr int L_GW = L_XS + 1280;
constexpr int L_QRED = L_GW + 640;
constexpr int L_FRED = L_QRED + 1024;
constexpr int L_PART = L_FRED + 2048;
constexpr int L_RAW = L_PART + 8 * 132;
constexpr int L_VIN = L_RAW + 8 * 132;
constexpr int L_RED = L_VIN + 384;
constexpr int L_G = L_RED + 1280;
constexpr int L_END = L_G + 4;

__device__ __forceinline__ float bflo(u32 u) { return __builtin_bit_cast(float, u << 16); }
__device__ __forceinline__ float bfhi(u32 u) { return __builtin_bit_cast(float, u & 0xffff0000u); }
__device__ __forceinline__ float rbf(float x) { return (float)(__bf16)x; }
__device__ __forceinline__ u32 pack_bf(float lo, float hi) {
  bf16x2 v = {(__bf16)lo, (__bf16)hi};
  return __builtin_bit_cast(u32, v);
}
__device__ __forceinline__ float dot2(u32 a, u32 b, float c) {
  return __builtin_amdgcn_fdot2_f32_bf16(__builtin_bit_cast(bf16x2, a), __builtin_bit_cast(bf16x2, b), c, false);
}
template <int CTRL> __device__ __forceinline__ float dpp(float x) {
  return __builtin_bit_cast(float, __builtin_amdgcn_mov_dpp(__builtin_bit_cast(int, x), CTRL, 0xf, 0xf, true));
}
__device__ __forceinline__ float row16_sum(float x) {
  x += dpp<0xB1>(x);
  x += dpp<0x4E>(x);
  x += dpp<0x141>(x);
  x += dpp<0x140>(x);
  return x;
}
__device__ __forceinline__ float xrow_sum(float x) {
  auto s = __builtin_amdgcn_permlane16_swap(__float_as_uint(x), __float_as_uint(x), false, false);
  x = __uint_as_float(s[0]) + __uint_as_float(s[1]);
  auto t = __builtin_amdgcn_permlane32_swap(__float_as_uint(x), __float_as_uint(x), false, false);
  return __uint_as_float(t[0]) + __uint_as_float(t[1]);
}
__device__ __forceinline__ float xrow_max(float x) {
  auto s = __builtin_amdgcn_permlane16_swap(__float_as_uint(x), __float_as_uint(x), false, false);
  x = fmaxf(__uint_as_float(s[0]), __uint_as_float(s[1]));
  auto t = __builtin_amdgcn_permlane32_swap(__float_as_uint(x), __float_as_uint(x), false, false);
  return fmaxf(__uint_as_float(t[0]), __uint_as_float(t[1]));
}
__device__ __forceinline__ u32x4 ldbuf(rsrc_t r, int voff, int soff, int aux_is_const_below) { return __builtin_amdgcn_raw_buffer_load_b128(r, voff, soff, AUX_NT); }
#define LD_NT(r, voff, soff) __builtin_amdgcn_raw_buffer_load_b128((r), (voff), (soff), AUX_NT)
#define LD_SC1(r, voff, soff) __builtin_amdgcn_raw_buffer_load_b128((r), (voff), (soff), AUX_SC1)
#define ST_SC1(v, r, voff, soff) __builtin_amdgcn_raw_buffer_store_b128((v), (r), (voff), (soff), AUX_SC1)
#define LDS_BARRIER() do { asm volatile("s_waitcnt lgkmcnt(0)" ::: "memory"); __builtin_amdgcn_s_barrier(); asm volatile("" ::: "memory"); } while (0)
__device__ __forceinline__ rsrc_t mk_rsrc(const void* p, unsigned bytes) { return __builtin_amdgcn_make_buffer_rsrc((void*)p, 0, bytes, 0x00020000); }
__device__ __forceinline__ u32x4 mk_unit(float a, float b, u32 tag) { u32x4 v = {__float_as_uint(a), tag, __float_as_uint(b), tag}; return v; }
__device__ __forceinline__ bool unit_ok(u32x4 v, u32 tag) { return v[1] == tag && v[3] == tag; }

constexpr unsigned SPIN_CAP = 1u << 18;

__global__ void __launch_bounds__(NTHR, 2)
fused_decode(const u32* __restrict__ x, const u32* __restrict__ kc, const u32* __restrict__ vc, const u32* __restrict__ wq,
             const u32* __restrict__ wo, const u32* __restrict__ wg, const u32* __restrict__ wu, const u32* __restrict__ wd,
             const u32* __restrict__ gw, u32* __restrict__ out, unsigned char* __restrict__ ws) {
  __shared__ __attribute__((aligned(16))) float lds[L_END];
  const int tid = threadIdx.x, lane = tid & 63, w = __builtin_amdgcn_readfirstlane(tid >> 6);
  const int r4 = lane >> 4, c = lane & 15;
  const int blk = blockIdx.x, xg = blk & 7, li = blk >> 3;
  const int h = xg * 4 + (li >> 3), j = li & 7, b = j >> 2, s = j & 3;
  const int fb = (h & 3) * 8 + (h >> 2);
  const bool has_ffn = fb < 27;
  const int bh = b * 32 + h;

  const rsrc_t rws = mk_rsrc(ws, WS_END);
  const rsrc_t rk = mk_rsrc((const unsigned char*)kc + (size_t)bh * (SK * DH * 2), SK * DH * 2);
  const rsrc_t rv = mk_rsrc((const unsigned char*)vc + (size_t)bh * (SK * DH * 2), SK * DH * 2);
  const rsrc_t rwq = mk_rsrc(wq, D * 4096 * 2);
  const rsrc_t rwg = mk_rsrc(wg, D * F * 2);
  const rsrc_t rwu = mk_rsrc(wu, D * F * 2);
  const rsrc_t rwo = mk_rsrc(wo, 4096 * D * 2);
  const rsrc_t rwd = mk_rsrc(wd, F * D * 2);
  const rsrc_t rx = mk_rsrc(x, 2 * D * 2);
  const rsrc_t rgw = mk_rsrc(gw, D * 2);

  constexpr int OOB = (int)0x80000000;
  u32 epoch = __builtin_amdgcn_raw_buffer_load_b32(rws, 0, WS_EPOCH, AUX_SC1);
  const u32x4 xv = __builtin_amdgcn_raw_buffer_load_b128(rx, tid * 16, 0, 0);
  const u32x4 gv = __builtin_amdgcn_raw_buffer_load_b128(rgw, tid * 16, 0, 0);
  u32x4 fw[3];
  const int fcc = tid & 7, fmat = w >> 2, frow0 = (tid & 255) >> 3;
  {
    const rsrc_t rf = mk_rsrc(fmat ? wu : wg, has_ffn ? D * F * 2 : 0);
    const int fo = ((80 * j + frow0) * F + 64 * fb + 8 * fcc) * 2;
    fw[0] = LD_NT(rf, fo, 0); fw[1] = LD_NT(rf, fo, 32 * F * 2);
    fw[2] = LD_NT(rf, frow0 < 16 ? fo : OOB, 64 * F * 2);
  }
  u32x4 qw[20];
  {
    const int rg = tid >> 4;
    const int qo = (20 * rg * 4096 + 128 * h + 8 * c) * 2;
#pragma unroll
    for (int i = 0; i < 20; i++) qw[i] = LD_NT(rwq, qo, i * 8192);
  }
  const int kvoff = lane * 16;
  const int kvbase = s * 16384 + w * 2048;
  u32x4 kr[RING], vr[RING];
#define SLOT_OFF(t) (kvbase + ((t) >> 1) * 65536 + ((t) & 1) * 1024)
#pragma unroll
  for (int i = 0; i < RING - 1; i++) { kr[i] = LD_NT(rk, kvoff, SLOT_OFF(i)); vr[i] = LD_NT(rv, kvoff, SLOT_OFF(i)); }
  __builtin_amdgcn_sched_barrier(0);
  asm volatile("" :: "v"(xv), "v"(gv));

  if (tid < 160) {
    float* dst = lds + L_XS + tid * 8;
    *(f32x4*)dst = f32x4{bflo(xv[0]), bfhi(xv[0]), bflo(xv[1]), bfhi(xv[1])}; *(f32x4*)(dst + 4) = f32x4{bflo(xv[2]), bfhi(xv[2]), bflo(xv[3]), bfhi(xv[3])};
  }
  if (tid < 80) {
    float* dst = lds + L_GW + tid * 8;
    *(f32x4*)dst = f32x4{bflo(gv[0]), bfhi(gv[0]), bflo(gv[1]), bfhi(gv[1])}; *(f32x4*)(dst + 4) = f32x4{bflo(gv[2]), bfhi(gv[2]), bflo(gv[3]), bfhi(gv[3])};
  }
  LDS_BARRIER();
  {
    float fa[2][8];
#pragma unroll
    for (int k = 0; k < 8; k++) fa[0][k] = fa[1][k] = 0.f;
#pragma unroll
    for (int i = 0; i < 3; i++) {
      const int row = 80 * j + frow0 + 32 * i;
      const int rr = row < 640 ? row : 639;
      const float x0 = lds[L_XS + rr], x1 = lds[L_XS + 640 + rr];
#pragma unroll
      for (int k = 0; k < 4; k++) {
        const float lo = bflo(fw[i][k]), hi = bfhi(fw[i][k]);
        fa[0][2 * k] = fmaf(x0, lo, fa[0][2 * k]); fa[0][2 * k + 1] = fmaf(x0, hi, fa[0][2 * k + 1]);
        fa[1][2 * k] = fmaf(x1, lo, fa[1][2 * k]); fa[1][2 * k + 1] = fmaf(x1, hi, fa[1][2 * k + 1]);
      }
    }
#pragma unroll
    for (int k = 0; k < 8; k++) {
      fa[0][k] = xrow_sum(fa[0][k] + dpp<0x128>(fa[0][k]));
      fa[1][k] = xrow_sum(fa[1][k] + dpp<0x128>(fa[1][k]));
    }
    if (lane < 8) {
      float* dst = lds + L_FRED + w * 128 + fcc * 8;
      *(f32x4*)(dst) = f32x4{fa[0][0], fa[0][1], fa[0][2], fa[0][3]}; *(f32x4*)(dst + 4) = f32x4{fa[0][4], fa[0][5], fa[0][6], fa[0][7]};
      *(f32x4*)(dst + 64) = f32x4{fa[1][0], fa[1][1], fa[1][2], fa[1][3]}; *(f32x4*)(dst + 68) = f32x4{fa[1][4], fa[1][5], fa[1][6], fa[1][7]};
    }
  }
  if (w >= 6) {
    const int bb = w - 6;
    float gs = 0.f;
#pragma unroll
    for (int i = 0; i < 10; i++) gs += rbf(lds[L_XS + bb * 640 + lane + 64 * i] * lds[L_GW + lane + 64 * i]);
    gs = row16_sum(gs); gs = xrow_sum(gs);
    const float g = rbf(1.f / (1.f + __expf(-rbf(gs))));
    if (lane == 0) { lds[L_G + bb] = g; lds[L_G + 2 + bb] = rbf(1.f - g); }
  }
  {
    float qa[8];
#pragma unroll
    for (int k = 0; k < 8; k++) qa[k] = 0.f;
    const float* xr = lds + L_XS + b * 640 + 20 * (tid >> 4);
#pragma unroll
    for (int i4 = 0; i4 < 5; i4++) {
      const f32x4 xx = *(const f32x4*)(xr + 4 * i4);
#pragma unroll
      for (int ii = 0; ii < 4; ii++) {
        const int i = i4 * 4 + ii;
#pragma unroll
        for (int k = 0; k < 4; k++) { qa[2 * k] = fmaf(xx[ii], bflo(qw[i][k]), qa[2 * k]); qa[2 * k + 1] = fmaf(xx[ii], bfhi(qw[i][k]), qa[2 * k + 1]); }
      }
    }
#pragma unroll
    for (int k = 0; k < 8; k++) qa[k] = xrow_sum(qa[k]);
    if (r4 == 0) {
      float* dst = lds + L_QRED + w * 128 + c * 8;
      *(f32x4*)(dst) = f32x4{qa[0], qa[1], qa[2], qa[3]}; *(f32x4*)(dst + 4) = f32x4{qa[4], qa[5], qa[6], qa[7]};
    }
  }
  LDS_BARRIER();
  u32 qp[4];
  {
    f32x4 s0 = {0, 0, 0, 0}, s1 = {0, 0, 0, 0};
#pragma unroll
    for (int ww = 0; ww < 8; ww++) { s0 += *(const f32x4*)(lds + L_QRED + ww * 128 + c * 8); s1 += *(const f32x4*)(lds + L_QRED + ww * 128 + c * 8 + 4); }
    qp[0] = pack_bf(s0[0], s0[1]); qp[1] = pack_bf(s0[2], s0[3]); qp[2] = pack_bf(s1[0], s1[1]); qp[3] = pack_bf(s1[2], s1[3]);
  }
  const u32 tag = __builtin_amdgcn_readfirstlane(epoch) + 1u;
  if (has_ffn && w < 2) {
    const int mat = lane >> 5, col = (lane & 31) * 2;
    f32x2 sum = {0, 0};
#pragma unroll
    for (int ww = 0; ww < 4; ww++) sum += *(const f32x2*)(lds + L_FRED + (mat * 4 + ww) * 128 + w * 64 + col);
    ST_SC1(mk_unit(sum[0], sum[1], tag), rws, tid * 16, WS_FFN + (h * 8 + j) * FFN_STRIDE);
  }

  constexpr float C2 = 0.08838834764831845f * 1.4426950408889634f;
  float m = -INFINITY, l = 0.f, acc[8];
#pragma unroll
  for (int k = 0; k < 8; k++) acc[k] = 0.f;
  u32x4 wt[6], fg[2];

#define STEP(i, tnext, issue) do { \
    if (issue) { kr[((i) + RING - 1) % RING] = LD_NT(rk, kvoff, SLOT_OFF(tnext)); vr[((i) + RING - 1) % RING] = LD_NT(rv, kvoff, SLOT_OFF(tnext)); } \
    __builtin_amdgcn_sched_barrier(0); \
    float sc = dot2(kr[i][0], qp[0], 0.f); sc = dot2(kr[i][1], qp[1], sc); sc = dot2(kr[i][2], qp[2], sc); sc = dot2(kr[i][3], qp[3], sc); \
    sc = row16_sum(sc) * C2; \
    const float mn = fmaxf(m, sc); \
    const float al = __builtin_amdgcn_exp2f(m - mn), p = __builtin_amdgcn_exp2f(sc - mn); \
    m = mn; l = fmaf(l, al, p); \
    _Pragma("unroll") for (int k = 0; k < 4; k++) { \
      acc[2 * k] = fmaf(p, bflo(vr[i][k]), acc[2 * k] * al); acc[2 * k + 1] = fmaf(p, bfhi(vr[i][k]), acc[2 * k + 1] * al); } \
    asm volatile("" : "+v"(acc[0]), "+v"(acc[1]), "+v"(acc[2]), "+v"(acc[3]), "+v"(acc[4]), "+v"(acc[5]), "+v"(acc[6]), "+v"(acc[7]), "+v"(l), "+v"(m)); \
    __builtin_amdgcn_sched_barrier(0); \
  } while (0)

  constexpr int NROUND = NSLOT / RING;
  static_assert(NSLOT % RING == 0, "ring");
  int rd = 0;
  for (; rd < NROUND - 1 - TAILAT; rd++) {
#pragma unroll
    for (int i = 0; i < RING; i++) STEP(i, rd * RING + i + RING - 1, true);
  }
  {
#pragma unroll
    for (int i = 0; i < 6; i++) {
      const int row0 = 24 * w + 4 * i;
      const bool isw = row0 < 128;
      const rsrc_t rt = mk_rsrc(isw ? wo : wd, isw ? 4096 * D * 2 : (has_ffn ? F * D * 2 : 0));
      const int grow = (isw ? 128 * h + row0 : 64 * fb + row0 - 128);
      wt[i] = LD_NT(rt, c < 10 ? (r4 * D + 8 * c) * 2 : OOB, (grow * D + 80 * j) * 2);
    }
    const rsrc_t rg = mk_rsrc(ws, has_ffn ? WS_END : 0);
    fg[0] = LD_SC1(rg, lane * 16, WS_FFN + (h * 8 + w) * FFN_STRIDE);
    fg[1] = LD_SC1(rg, lane * 16 + 1024, WS_FFN + (h * 8 + w) * FFN_STRIDE);
    __builtin_amdgcn_sched_barrier(0);
  }
  for (; rd < NROUND - 1; rd++) {
#pragma unroll
    for (int i = 0; i < RING; i++) STEP(i, rd * RING + i + RING - 1, true);
  }
  STEP(0, NSLOT - 1, true);
#pragma unroll
  for (int i = 1; i < RING; i++) STEP(i, 0, false);

  {
    const float M = xrow_max(m);
    const float f = __builtin_amdgcn_exp2f(m - M);
    l = xrow_sum(l * f);
#pragma unroll
    for (int k = 0; k < 8; k++) acc[k] = xrow_sum(acc[k] * f);
    if (r4 == 0) {
      float* dst = lds + L_PART + w * 132 + c * 8;
      *(f32x4*)(dst) = f32x4{acc[0], acc[1], acc[2], acc[3]}; *(f32x4*)(dst + 4) = f32x4{acc[4], acc[5], acc[6], acc[7]};
      if (c == 0) { lds[L_PART + w * 132 + 128] = M; lds[L_PART + w * 132 + 129] = l; }
    }
  }
  bool ok = true;
  if (has_ffn) {
    unsigned spins = 0;
    while (!__all(unit_ok(fg[0], tag) && unit_ok(fg[1], tag))) {
      if (++spins > SPIN_CAP) { ok = false; break; }
      __builtin_amdgcn_s_sleep(4);
      int vo = lane * 16;
      asm volatile("" : "+v"(vo) :: "memory");
      fg[0] = LD_SC1(rws, vo, WS_FFN + (h * 8 + w) * FFN_STRIDE);
      fg[1] = LD_SC1(rws, vo + 1024, WS_FFN + (h * 8 + w) * FFN_STRIDE);
    }
    *(f32x2*)(lds + L_FRED + w * 256 + lane * 2) = f32x2{__uint_as_float(fg[0][0]), __uint_as_float(fg[0][2])};
    *(f32x2*)(lds + L_FRED + w * 256 + 128 + lane * 2) = f32x2{__uint_as_float(fg[1][0]), __uint_as_float(fg[1][2])};
  }
  LDS_BARRIER();
  if (w == 0) {
    float mw[8], M = -INFINITY;
#pragma unroll
    for (int ww = 0; ww < 8; ww++) { mw[ww] = lds[L_PART + ww * 132 + 128]; M = fmaxf(M, mw[ww]); }
    float L = 0.f, o0 = 0.f, o1 = 0.f;
#pragma unroll
    for (int ww = 0; ww < 8; ww++) {
      const float e = __builtin_amdgcn_exp2f(mw[ww] - M);
      const f32x2 a = *(const f32x2*)(lds + L_PART + ww * 132 + lane * 2);
      L = fmaf(e, lds[L_PART + ww * 132 + 129], L); o0 = fmaf(e, a[0], o0); o1 = fmaf(e, a[1], o1);
    }
    const int so = WS_HOP1 + (h * 8 + j) * HOP1_STRIDE;
    ST_SC1(mk_unit(o0, o1, tag), rws, lane * 16, so);
    if (lane == 0) ST_SC1(mk_unit(M, L, tag), rws, 1024, so);
  } else if (w <= 2) {
    const int bb = w - 1;
    float g = 0.f, u = 0.f;
    if (has_ffn) {
#pragma unroll
      for (int ww = 0; ww < 8; ww++) { g += lds[L_FRED + ww * 256 + bb * 128 + lane]; u += lds[L_FRED + ww * 256 + bb * 128 + 64 + lane]; }
    }
    g = rbf(g); u = rbf(u);
    const float t = 0.7978845608028654f * (g + 0.044715f * g * g * g);
    const float th = 1.f - 2.f / (1.f + __expf(2.f * t));
    const float hid = rbf(rbf(0.5f * g * (1.f + th)) * u);
    lds[L_VIN + (128 + lane) * 2 + bb] = lds[L_G + 2 + bb] * hid;
  }
  {
    const int so = WS_HOP1 + (h * 8 + w) * HOP1_STRIDE;
    u32x4 a, ml;
    unsigned spins = 0;
    while (true) {
      int vo = lane * 16, vz = 1024;
      asm volatile("" : "+v"(vo), "+v"(vz) :: "memory");
      a = LD_SC1(rws, vo, so);
      ml = LD_SC1(rws, vz, so);
      if (__all(unit_ok(a, tag) && unit_ok(ml, tag))) break;
      if (++spins > SPIN_CAP) { ok = false; break; }
      __builtin_amdgcn_s_sleep(2);
      asm volatile("" ::: "memory");
    }
    *(f32x2*)(lds + L_RAW + w * 132 + lane * 2) = f32x2{__uint_as_float(a[0]), __uint_as_float(a[2])};
    if (lane == 0) { lds[L_RAW + w * 132 + 128] = __uint_as_float(ml[0]); lds[L_RAW + w * 132 + 129] = __uint_as_float(ml[2]); }
  }
  LDS_BARRIER();
  if (tid < 256) {
    const int bb = tid >> 7, d = tid & 127;
    const float* rp = lds + L_RAW + bb * 4 * 132;
    const float m0 = rp[128], m1 = rp[132 + 128], m2 = rp[264 + 128], m3 = rp[396 + 128];
    const float M = fmaxf(fmaxf(m0, m1), fmaxf(m2, m3));
    const float e0 = __builtin_amdgcn_exp2f(m0 - M), e1 = __builtin_amdgcn_exp2f(m1 - M), e2 = __builtin_amdgcn_exp2f(m2 - M), e3 = __builtin_amdgcn_exp2f(m3 - M);
    const float L = e0 * rp[129] + e1 * rp[132 + 129] + e2 * rp[264 + 129] + e3 * rp[396 + 129];
    const float o = (e0 * rp[d] + e1 * rp[132 + d] + e2 * rp[264 + d] + e3 * rp[396 + d]) / L;
    lds[L_VIN + d * 2 + bb] = lds[L_G + bb] * rbf(o);
  }
  LDS_BARRIER();
  {
    float a0[8], a1[8];
#pragma unroll
    for (int k = 0; k < 8; k++) a0[k] = a1[k] = 0.f;
#pragma unroll
    for (int i = 0; i < 6; i++) {
      const f32x2 vi = *(const f32x2*)(lds + L_VIN + (24 * w + 4 * i + r4) * 2);
#pragma unroll
      for (int k = 0; k < 4; k++) {
        const float lo = bflo(wt[i][k]), hi = bfhi(wt[i][k]);
        a0[2 * k] = fmaf(vi[0], lo, a0[2 * k]); a0[2 * k + 1] = fmaf(vi[0], hi, a0[2 * k + 1]);
        a1[2 * k] = fmaf(vi[1], lo, a1[2 * k]); a1[2 * k + 1] = fmaf(vi[1], hi, a1[2 * k + 1]);
      }
    }
#pragma unroll
    for (int k = 0; k < 8; k++) { a0[k] = xrow_sum(a0[k]); a1[k] = xrow_sum(a1[k]); }
    if (r4 == 0 && c < 10) {
      float* dst = lds + L_RED + w * 160 + c * 8;
      *(f32x4*)(dst) = f32x4{a0[0], a0[1], a0[2], a0[3]}; *(f32x4*)(dst + 4) = f32x4{a0[4], a0[5], a0[6], a0[7]};
      *(f32x4*)(dst + 80) = f32x4{a1[0], a1[1], a1[2], a1[3]}; *(f32x4*)(dst + 84) = f32x4{a1[4], a1[5], a1[6], a1[7]};
    }
  }
  LDS_BARRIER();
  if (tid < 80) {
    f32x2 sum = {0, 0};
#pragma unroll
    for (int ww = 0; ww < 8; ww++) sum += *(const f32x2*)(lds + L_RED + ww * 160 + tid * 2);
    ST_SC1(mk_unit(sum[0], sum[1], tag), rws, tid * 16, WS_HOP2 + (j * 32 + h) * HOP2_STRIDE);
  }
  if (w == 0 && h < 20) {
    const int bb = h / 10, cg = h % 10;
    const int hp = lane >> 1, half = lane & 1;
    const int vo = hp * HOP2_STRIDE + (bb * 40 + cg * 4 + half * 2) * 16;
    const int so = WS_HOP2 + j * 32 * HOP2_STRIDE;
    u32x4 a0, a1;
    unsigned spins = 0;
    while (true) {
      int vv = vo;
      asm volatile("" : "+v"(vv) :: "memory");
      a0 = LD_SC1(rws, vv, so);
      a1 = LD_SC1(rws, vv + 16, so);
      if (__all(unit_ok(a0, tag) && unit_ok(a1, tag))) break;
      if (++spins > SPIN_CAP) { ok = false; break; }
      __builtin_amdgcn_s_sleep(1);
      asm volatile("" ::: "memory");
    }
    float v0 = __uint_as_float(a0[0]), v1 = __uint_as_float(a0[2]), v2 = __uint_as_float(a1[0]), v3 = __uint_as_float(a1[2]);
#pragma unroll
    for (int o = 2; o < 64; o <<= 1) { v0 += __shfl_xor(v0, o); v1 += __shfl_xor(v1, o); v2 += __shfl_xor(v2, o); v3 += __shfl_xor(v3, o); }
    if (lane < 2) {
      u32x2 r = {pack_bf(v0, v1), pack_bf(v2, v3)};
      *(u32x2*)(out + (bb * 640 + 80 * j + 8 * cg + 4 * half) / 2) = r;
    }
    if (blk == 0 && lane == 0) __builtin_amdgcn_raw_buffer_store_b32(tag, rws, 0, WS_EPOCH, AUX_SC1);
  }
  (void)ok;
}

extern "C" void kernel_launch(void* const* d_in, const int* in_sizes, int n_in, void* d_out, int out_size, void* d_ws, size_t ws_size, hipStream_t stream) {
  fused_decode<<<NWG, NTHR, 0, stream>>>((const u32*)d_in[0], (const u32*)d_in[1], (const u32*)d_in[2], (const u32*)d_in[3], (const u32*)d_in[4],
                                         (const u32*)d_in[6], (const u32*)d_in[7], (const u32*)d_in[8], (const u32*)d_in[9], (u32*)d_out, (unsigned char*)d_ws);
}
```

```cpp
#include <hip/hip_runtime.h>
#include <hip/hip_bf16.h>
#include <stdint.h>
#include <stdio.h>

typedef unsigned u32;
using u32x4 = __attribute__((ext_vector_type(4))) unsigned;
using u32x2 = __attribute__((ext_vector_type(2))) unsigned;
using f32x4 = __attribute__((ext_vector_type(4))) float;
using f32x2 = __attribute__((ext_vector_type(2))) float;
using bf16x2 = __attribute__((ext_vector_type(2))) __bf16;
using bf16x8 = __attribute__((ext_vector_type(8))) __bf16;
typedef __amdgpu_buffer_rsrc_t rsrc_t;
#define GAS __attribute__((address_space(1)))

#define RING 2
#define S0BAR 1
#define ROT_EXPR (48 * b)
#define WT_EARLY 1

constexpr int D = 640, H = 32, SK = 8192, DH = 128, F = 1728;
constexpr int NWG = 256, NTHR = 512;
constexpr int AUX_NT = 2, AUX_SC1 = 16;
constexpr int NSLOT = 512;
constexpr int OOB = (int)0x80000000;

constexpr unsigned WS_EPOCH = 0;
constexpr unsigned WS_QP = 4096, QP_STRIDE = 2048;
constexpr unsigned WS_FFN = WS_QP + 256 * QP_STRIDE, FFN_STRIDE = 2048;
constexpr unsigned WS_HOP1 = WS_FFN + 256 * FFN_STRIDE, HOP1_STRIDE = 1152;
constexpr unsigned WS_HOP2 = WS_HOP1 + 256 * HOP1_STRIDE, HOP2_STRIDE = 1280;
constexpr unsigned WS_END = WS_HOP2 + 256 * HOP2_STRIDE;

constexpr int L_QRED = 0;
constexpr int L_FRED = L_QRED + 2048;
constexpr int L_Q = L_FRED + 2048;
constexpr int L_PART = L_Q + 64;
constexpr int L_VV = L_PART + 8 * 132;
constexpr int L_RED = L_VV + 384;
constexpr int L_G = L_RED + 1280;
constexpr int L_CTR = L_G + 4;
constexpr int L_END = L_CTR + 4;

__device__ __forceinline__ float bflo(u32 u) { return __builtin_bit_cast(float, u << 16); }
__device__ __forceinline__ float bfhi(u32 u) { return __builtin_bit_cast(float, u & 0xffff0000u); }
__device__ __forceinline__ float rbf(float x) { return (float)(__bf16)x; }
__device__ __forceinline__ u32 pack_bf(float lo, float hi) {
  bf16x2 v = {(__bf16)lo, (__bf16)hi};
  return __builtin_bit_cast(u32, v);
}
__device__ __forceinline__ float dot2(u32 a, u32 b, float c) {
  return __builtin_amdgcn_fdot2_f32_bf16(__builtin_bit_cast(bf16x2, a), __builtin_bit_cast(bf16x2, b), c, false);
}
__device__ __forceinline__ f32x4 mfma(u32x4 a, u32x4 b, f32x4 c) {
  return __builtin_amdgcn_mfma_f32_16x16x32_bf16(__builtin_bit_cast(bf16x8, a), __builtin_bit_cast(bf16x8, b), c, 0, 0, 0);
}
template <int CTRL> __device__ __forceinline__ float dpp(float x) {
  return __builtin_bit_cast(float, __builtin_amdgcn_mov_dpp(__builtin_bit_cast(int, x), CTRL, 0xf, 0xf, true));
}
__device__ __forceinline__ float row16_sum(float x) {
  x += dpp<0xB1>(x);
  x += dpp<0x4E>(x);
  x += dpp<0x141>(x);
  x += dpp<0x140>(x);
  return x;
}
__device__ __forceinline__ float xrow_sum(float x) {
  auto s = __builtin_amdgcn_permlane16_swap(__float_as_uint(x), __float_as_uint(x), false, false);
  x = __uint_as_float(s[0]) + __uint_as_float(s[1]);
  auto t = __builtin_amdgcn_permlane32_swap(__float_as_uint(x), __float_as_uint(x), false, false);
  return __uint_as_float(t[0]) + __uint_as_float(t[1]);
}
__device__ __forceinline__ float xrow_max(float x) {
  auto s = __builtin_amdgcn_permlane16_swap(__float_as_uint(x), __float_as_uint(x), false, false);
  x = fmaxf(__uint_as_float(s[0]), __uint_as_float(s[1]));
  auto t = __builtin_amdgcn_permlane32_swap(__float_as_uint(x), __float_as_uint(x), false, false);
  return fmaxf(__uint_as_float(t[0]), __uint_as_float(t[1]));
}
#define LD_NT(r, voff, soff) __builtin_amdgcn_raw_buffer_load_b128((r), (voff), (soff), AUX_NT)
#define LD_SC1(r, voff, soff) __builtin_amdgcn_raw_buffer_load_b128((r), (voff), (soff), AUX_SC1)
#define ST_SC1(v, r, voff, soff) __builtin_amdgcn_raw_buffer_store_b128((v), (r), (voff), (soff), AUX_SC1)
#define LDS_BARRIER() do { asm volatile("s_waitcnt lgkmcnt(0)" ::: "memory"); __builtin_amdgcn_s_barrier(); asm volatile("" ::: "memory"); } while (0)
__device__ __forceinline__ rsrc_t mk_rsrc(const void* p, unsigned bytes) { return __builtin_amdgcn_make_buffer_rsrc((void*)p, 0, bytes, 0x00020000); }
__device__ __forceinline__ u32x4 mk_unit(float a, float b, u32 tag) { u32x4 v = {__float_as_uint(a), tag, __float_as_uint(b), tag}; return v; }
__device__ __forceinline__ bool unit_ok(u32x4 v, u32 tag) { return v[1] == tag && v[3] == tag; }
__device__ __forceinline__ u32x4 onehot(u32 dup, const u32 (&msk)[4]) { u32x4 a = {dup & msk[0], dup & msk[1], dup & msk[2], dup & msk[3]}; return a; }

constexpr unsigned SPIN_CAP = 1u << 18;
#define STAMP(k) do { } while (0)
#define STAMP_DECL do { } while (0)
#define STAMP_FLUSH() do { } while (0)

__global__ void __launch_bounds__(NTHR, 2)
fused_decode(const u32* __restrict__ x, const u32* __restrict__ kc, const u32* __restrict__ vc, const u32* __restrict__ wq,
             const u32* __restrict__ wo, const u32* __restrict__ wg, const u32* __restrict__ wu, const u32* __restrict__ wd,
             const u32* __restrict__ gw, u32* __restrict__ out, unsigned char* __restrict__ ws) {
  __shared__ __attribute__((aligned(16))) float lds[L_END];
  u32* const ldsu = (u32*)lds;
  const int tid = threadIdx.x, lane = tid & 63, w = __builtin_amdgcn_readfirstlane(tid >> 6);
  const int r4 = lane >> 4, c = lane & 15;
  const int blk = blockIdx.x, xg = blk & 7, li = blk >> 3;
  const int h = xg * 4 + (li >> 3), j = li & 7, b = j >> 2, s = j & 3;
  const int fb = (h & 3) * 8 + (h >> 2);
  const bool has_ffn = fb < 27;
  const int bh = b * 32 + h;
  STAMP_DECL;
  STAMP(0);

  const rsrc_t rws = mk_rsrc(ws, WS_END);
  const rsrc_t rk = mk_rsrc((const unsigned char*)kc + (size_t)bh * (SK * DH * 2), SK * DH * 2);
  const rsrc_t rv = mk_rsrc((const unsigned char*)vc + (size_t)bh * (SK * DH * 2), SK * DH * 2);

  u32 epoch = __builtin_amdgcn_raw_buffer_load_b32(rws, 0, WS_EPOCH, AUX_SC1);
  u32 xs[3];
  u32x4 qw[3], fw[3];
  {
    const rsrc_t rx = mk_rsrc(x, 2 * D * 2);
    const rsrc_t rwq = mk_rsrc(wq, D * 4096 * 2);
    const int row0 = 80 * j + 10 * w + r4;
#pragma unroll
    for (int u = 0; u < 3; u++) {
      const bool ok = (u < 2) || (r4 < 2);
      xs[u] = __builtin_amdgcn_raw_buffer_load_b16(rx, ok ? ((c >> 3) * D + row0 + 4 * u) * 2 : OOB, 0, AUX_NT);
    }
#pragma unroll
    for (int u = 0; u < 3; u++) {
      const bool ok = (u < 2) || (r4 < 2);
      qw[u] = LD_NT(rwq, ok ? ((row0 + 4 * u) * 4096 + 128 * h + 8 * c) * 2 : OOB, 0);
    }
    __builtin_amdgcn_sched_barrier(0); __builtin_amdgcn_s_barrier(); __builtin_amdgcn_sched_barrier(0);
    const int fbc = has_ffn ? fb : 0;
    const GAS unsigned char* fbase = (const GAS unsigned char*)((c < 8) ? wg : wu) + (64 * fbc + 8 * (c & 7)) * 2;
#pragma unroll
    for (int u = 0; u < 3; u++) {
      const int row = (u < 2 || r4 < 2) ? row0 + 4 * u : row0;
      fw[u] = __builtin_nontemporal_load((const GAS u32x4*)(fbase + (size_t)row * (F * 2)));
    }
  }
  __builtin_amdgcn_sched_barrier(0);
  u32 msk[4];
#pragma unroll
  for (int k = 0; k < 4; k++) msk[k] = (k == ((lane & 7) >> 1)) ? ((lane & 1) ? 0xffff0000u : 0x0000ffffu) : 0u;
  if (tid == 0) ldsu[L_CTR] = 8 * RING;
  u32x4 xa[3];
  {
    f32x4 qa = {0, 0, 0, 0};
#pragma unroll
    for (int u = 0; u < 3; u++) { xa[u] = onehot(xs[u] * 0x10001u, msk); qa = mfma(xa[u], qw[u], qa); }
    *(f32x4*)(lds + L_QRED + w * 256 + (lane >> 5) * 128 + 8 * c + 4 * (r4 & 1)) = qa;
  }
  STAMP(1);
  LDS_BARRIER();
  STAMP(2);
  const u32 tag = __builtin_amdgcn_readfirstlane(epoch) + 1u;
  if (w < 2) {
    f32x2 sum = {0, 0};
#pragma unroll
    for (int ww = 0; ww < 8; ww++) sum += *(const f32x2*)(lds + L_QRED + ww * 256 + tid * 2);
    ST_SC1(mk_unit(sum[0], sum[1], tag), rws, tid * 16, WS_QP + (h * 8 + j) * QP_STRIDE);
  }
  u32x4 wt[6];
  const int wtoff = c < 10 ? (r4 * D + 8 * c) * 2 : OOB;
#define LOAD_WT() do { \
    const rsrc_t rwo = mk_rsrc(wo, 4096 * D * 2), rwd = mk_rsrc(wd, has_ffn ? F * D * 2 : 0); \
    _Pragma("unroll") for (int i = 0; i < 4; i++) wt[i] = LD_NT(rwo, wtoff, ((128 * h + 16 * w + 4 * i) * D + 80 * j) * 2); \
    _Pragma("unroll") for (int i = 0; i < 2; i++) wt[4 + i] = LD_NT(rwd, wtoff, ((64 * fb + 8 * w + 4 * i) * D + 80 * j) * 2); } while (0)
  if (WT_EARLY) LOAD_WT();
  u32x4 gx0, gx1, gg0, gg1;
  if (w >= 6) {
    const rsrc_t rxb = mk_rsrc((const unsigned char*)x + (w - 6) * D * 2, D * 2), rgw = mk_rsrc(gw, D * 2);
    gx0 = LD_NT(rxb, lane * 16, 0); gx1 = LD_NT(rxb, lane * 16 + 1024, 0);
    gg0 = LD_NT(rgw, lane * 16, 0); gg1 = LD_NT(rgw, lane * 16 + 1024, 0);
  }
  u32x4 qg[8];
  if (w == 0) {
#pragma unroll
    for (int p = 0; p < 8; p++) qg[p] = LD_SC1(rws, lane * 16 + b * 1024, WS_QP + (h * 8 + p) * QP_STRIDE);
  }
  const int kvoff = lane * 16;
  const int kvbase = s * 16384;
  u32x4 kr[RING], vr[RING];
  const int rot = ROT_EXPR;
#define SLOT_OFF(t) (kvbase + ((((t) + rot) & 511) >> 4) * 65536 + ((t) & 15) * 1024)
#pragma unroll
  for (int i = 0; i < RING - 1; i++) { kr[i] = LD_NT(rk, kvoff, SLOT_OFF(w + 8 * i)); vr[i] = LD_NT(rv, kvoff, SLOT_OFF(w + 8 * i)); }
  __builtin_amdgcn_sched_barrier(0);
  STAMP(3);
  {
    f32x4 fa = {0, 0, 0, 0};
#pragma unroll
    for (int u = 0; u < 3; u++) fa = mfma(xa[u], fw[u], fa);
    *(f32x4*)(lds + L_FRED + w * 256 + (lane >> 5) * 128 + 8 * c + 4 * (r4 & 1)) = fa;
  }
  if (w >= 6) {
    float gs = 0.f;
#pragma unroll
    for (int k = 0; k < 4; k++) {
      gs += rbf(bflo(gx0[k]) * bflo(gg0[k])) + rbf(bfhi(gx0[k]) * bfhi(gg0[k]));
      gs += rbf(bflo(gx1[k]) * bflo(gg1[k])) + rbf(bfhi(gx1[k]) * bfhi(gg1[k]));
    }
    gs = xrow_sum(row16_sum(gs));
    const float g = rbf(1.f / (1.f + __expf(-rbf(gs))));
    if (lane == 0) { lds[L_G + w - 6] = g; lds[L_G + 2 + w - 6] = rbf(1.f - g); }
  }
  if (w == 0) {
    unsigned spins = 0;
    while (true) {
      bool ok = true;
#pragma unroll
      for (int p = 0; p < 8; p++) ok = ok && unit_ok(qg[p], tag);
      if (__all(ok) || ++spins > SPIN_CAP) break;
      __builtin_amdgcn_s_sleep(1);
      int vo = lane * 16 + b * 1024;
      asm volatile("" : "+v"(vo) :: "memory");
#pragma unroll
      for (int p = 0; p < 8; p++) qg[p] = LD_SC1(rws, vo, WS_QP + (h * 8 + p) * QP_STRIDE);
    }
    float q0 = 0.f, q1 = 0.f;
#pragma unroll
    for (int p = 0; p < 8; p++) { q0 += __uint_as_float(qg[p][0]); q1 += __uint_as_float(qg[p][2]); }
    ldsu[L_Q + lane] = pack_bf(q0, q1);
  }
  STAMP(4);
  LDS_BARRIER();
  const u32x4 qp = *(const u32x4*)(ldsu + L_Q + c * 4);
  asm volatile("" :: "v"(qp));
  if (w == 2 || w == 3) {
    float g = 0.f, u = 0.f;
#pragma unroll
    for (int ww = 0; ww < 8; ww++) { g += lds[L_FRED + ww * 256 + (w - 2) * 128 + lane]; u += lds[L_FRED + ww * 256 + (w - 2) * 128 + 64 + lane]; }
    if (has_ffn) ST_SC1(mk_unit(g, u, tag), rws, (tid - 128) * 16, WS_FFN + (h * 8 + j) * FFN_STRIDE);
  }
  STAMP(5);

  constexpr float C2 = 0.08838834764831845f * 1.4426950408889634f;
  float mraw = -INFINITY, mc = -INFINITY, l = 0.f;
  f32x4 acc = {0, 0, 0, 0};
  int tn = w + 8 * (RING - 1);
  int tq[RING];
#pragma unroll
  for (int i = 0; i < RING; i++) tq[i] = 0;
  const u32 ctr_addr = (u32)(L_CTR * 4), one = 1u;
  for (;;) {
#pragma unroll
    for (int i = 0; i < RING; i++) {
      u32 pf; unsigned long long ex;
      asm volatile("s_mov_b64 %1, exec\n\ts_mov_b64 exec, 1\n\tds_add_rtn_u32 %0, %2, %3\n\ts_mov_b64 exec, %1" : "=&v"(pf), "=&s"(ex) : "v"(ctr_addr), "v"(one) : "memory");
      const int e = (i + RING - 1) % RING;
      const int so = tn < NSLOT ? SLOT_OFF(tn) : OOB;
      kr[e] = LD_NT(rk, kvoff, so); vr[e] = LD_NT(rv, kvoff, so);
      tq[e] = tn;
      __builtin_amdgcn_sched_barrier(0);
      if (tq[i] >= NSLOT) { asm volatile("s_waitcnt lgkmcnt(0)" :: "v"(pf) : "memory"); goto loop_done; }
      float sc = dot2(kr[i][0], qp[0], 0.f); sc = dot2(kr[i][1], qp[1], sc); sc = dot2(kr[i][2], qp[2], sc); sc = dot2(kr[i][3], qp[3], sc);
      sc = row16_sum(sc);
      if (__builtin_amdgcn_ballot_w64(sc > mraw) != 0) {
        const float mn = xrow_max(fmaxf(sc, mraw));
        const float mnc = mn * C2;
        const float al = __builtin_amdgcn_exp2f(mc - mnc);
        acc *= al; l *= al; mraw = mn; mc = mnc;
      }
      const float p = __builtin_amdgcn_exp2f(fmaf(sc, C2, -mc));
      l += p;
      acc = mfma(onehot(pack_bf(p, p), msk), vr[i], acc);
      asm volatile("" : "+v"(acc), "+v"(l));
      asm volatile("s_waitcnt lgkmcnt(0)\n\tv_readfirstlane_b32 %0, %1" : "=s"(tn) : "v"(pf) : "memory");
      __builtin_amdgcn_sched_barrier(0);
    }
  }
loop_done:
  STAMP(6);
  if (!WT_EARLY) LOAD_WT();

  l = xrow_sum(l);
  if (lane < 32) *(f32x4*)(lds + L_PART + w * 132 + 8 * c + 4 * r4) = acc;
  if (lane == 0) { lds[L_PART + w * 132 + 128] = mc; lds[L_PART + w * 132 + 129] = l; }
  LDS_BARRIER();
  STAMP(7);
  if (w == 0) {
    float mw[8], M = -INFINITY;
#pragma unroll
    for (int ww = 0; ww < 8; ww++) { mw[ww] = lds[L_PART + ww * 132 + 128]; M = fmaxf(M, mw[ww]); }
    float L = 0.f, o0 = 0.f, o1 = 0.f;
#pragma unroll
    for (int ww = 0; ww < 8; ww++) {
      const float e = __builtin_amdgcn_exp2f(mw[ww] - M);
      const f32x2 a = *(const f32x2*)(lds + L_PART + ww * 132 + lane * 2);
      L = fmaf(e, lds[L_PART + ww * 132 + 129], L); o0 = fmaf(e, a[0], o0); o1 = fmaf(e, a[1], o1);
    }
    const int so = WS_HOP1 + (h * 8 + j) * HOP1_STRIDE;
    ST_SC1(mk_unit(o0, o1, tag), rws, lane * 16, so);
    if (lane == 0) ST_SC1(mk_unit(M, L, tag), rws, 1024, so);
  } else if (w <= 2) {
    const int bb = w - 1;
    u32x4 a[4], ml[4];
    unsigned spins = 0;
    while (true) {
      int vo = lane * 16, vz = 1024;
      asm volatile("" : "+v"(vo), "+v"(vz) :: "memory");
      bool ok = true;
#pragma unroll
      for (int p = 0; p < 4; p++) { const int so = WS_HOP1 + (h * 8 + bb * 4 + p) * HOP1_STRIDE; a[p] = LD_SC1(rws, vo, so); ml[p] = LD_SC1(rws, vz, so); }
#pragma unroll
      for (int p = 0; p < 4; p++) ok = ok && unit_ok(a[p], tag) && unit_ok(ml[p], tag);
      if (__all(ok) || ++spins > SPIN_CAP) break;
      __builtin_amdgcn_s_sleep(1);
    }
    float M = -INFINITY;
#pragma unroll
    for (int p = 0; p < 4; p++) M = fmaxf(M, __uint_as_float(ml[p][0]));
    float L = 0.f, o0 = 0.f, o1 = 0.f;
#pragma unroll
    for (int p = 0; p < 4; p++) {
      const float e = __builtin_amdgcn_exp2f(__uint_as_float(ml[p][0]) - M);
      L = fmaf(e, __uint_as_float(ml[p][2]), L); o0 = fmaf(e, __uint_as_float(a[p][0]), o0); o1 = fmaf(e, __uint_as_float(a[p][2]), o1);
    }
    const float inv = 1.f / L;
    o0 *= inv; o1 *= inv;
    *(u32x2*)(ldsu + L_VV + bb * 192 + lane * 2) = u32x2{pack_bf(o0, o0), pack_bf(o1, o1)};
  } else if (w <= 4) {
    const int bb = w - 3;
    float g = 0.f, u = 0.f;
    if (has_ffn) {
      u32x4 a[8];
      unsigned spins = 0;
      while (true) {
        int vo = lane * 16 + bb * 1024;
        asm volatile("" : "+v"(vo) :: "memory");
        bool ok = true;
#pragma unroll
        for (int p = 0; p < 8; p++) a[p] = LD_SC1(rws, vo, WS_FFN + (h * 8 + p) * FFN_STRIDE);
#pragma unroll
        for (int p = 0; p < 8; p++) ok = ok && unit_ok(a[p], tag);
        if (__all(ok) || ++spins > SPIN_CAP) break;
        __builtin_amdgcn_s_sleep(1);
      }
#pragma unroll
      for (int p = 0; p < 8; p++) { g += __uint_as_float(a[p][0]); u += __uint_as_float(a[p][2]); }
    }
    g = rbf(g); u = rbf(u);
    const float t = 0.7978845608028654f * (g + 0.044715f * g * g * g);
    const float th = 1.f - 2.f / (1.f + __expf(2.f * t));
    const float hid = rbf(0.5f * g * (1.f + th)) * u;
    ldsu[L_VV + bb * 192 + 128 + lane] = pack_bf(hid, hid);
  }
  LDS_BARRIER();
  STAMP(8);
  {
    f32x4 ao = {0, 0, 0, 0}, ad = {0, 0, 0, 0};
    const u32* vv = ldsu + L_VV + (c >> 3) * 192;
#pragma unroll
    for (int i = 0; i < 4; i++) ao = mfma(onehot(vv[16 * w + 4 * i + r4], msk), wt[i], ao);
#pragma unroll
    for (int i = 0; i < 2; i++) ad = mfma(onehot(vv[128 + 8 * w + 4 * i + r4], msk), wt[4 + i], ad);
    const int bb = lane >> 5;
    const float g = lds[L_G + bb], g1 = lds[L_G + 2 + bb];
    if (c < 10) *(f32x4*)(lds + L_RED + w * 160 + bb * 80 + 8 * c + 4 * (r4 & 1)) = g * ao + g1 * ad;
  }
  LDS_BARRIER();
  STAMP(9);
  if (tid < 80) {
    f32x2 sum = {0, 0};
#pragma unroll
    for (int ww = 0; ww < 8; ww++) sum += *(const f32x2*)(lds + L_RED + ww * 160 + tid * 2);
    ST_SC1(mk_unit(sum[0], sum[1], tag), rws, tid * 16, WS_HOP2 + (j * 32 + h) * HOP2_STRIDE);
  }
  if (w == 0 && h < 20) {
    const int bb = h / 10, cg = h % 10;
    const int hp = lane >> 1, half = lane & 1;
    const int vo = hp * HOP2_STRIDE + (bb * 40 + cg * 4 + half * 2) * 16;
    const int so = WS_HOP2 + j * 32 * HOP2_STRIDE;
    u32x4 a0, a1;
    unsigned spins = 0;
    while (true) {
      int vv = vo;
      asm volatile("" : "+v"(vv) :: "memory");
      a0 = LD_SC1(rws, vv, so);
      a1 = LD_SC1(rws, vv + 16, so);
      if (__all(unit_ok(a0, tag) && unit_ok(a1, tag)) || ++spins > SPIN_CAP) break;
      __builtin_amdgcn_s_sleep(1);
    }
    float v[4] = {__uint_as_float(a0[0]), __uint_as_float(a0[2]), __uint_as_float(a1[0]), __uint_as_float(a1[2])};
#pragma unroll
    for (int k = 0; k < 4; k++) {
      v[k] += dpp<0x4E>(v[k]);
      v[k] += dpp<0x124>(v[k]);
      v[k] += dpp<0x128>(v[k]);
      v[k] = xrow_sum(v[k]);
    }
    if (lane < 2) {
      u32x2 r = {pack_bf(v[0], v[1]), pack_bf(v[2], v[3])};
      *(u32x2*)(out + (bb * 640 + 80 * j + 8 * cg + 4 * half) / 2) = r;
    }
    if (blk == 0 && lane == 0) __builtin_amdgcn_raw_buffer_store_b32(tag, rws, 0, WS_EPOCH, AUX_SC1);
  }
  STAMP(10);
  STAMP_FLUSH();
}

extern "C" void kernel_launch(void* const* d_in, const int* in_sizes, int n_in, void* d_out, int out_size, void* d_ws, size_t ws_size, hipStream_t stream) {
  fprintf(stderr, "PTRS:"); for (int i = 0; i < n_in; i++) fprintf(stderr, " in%d=%p", i, d_in[i]); fprintf(stderr, " out=%p ws=%p\n", d_out, d_ws);
  fused_decode<<<NWG, NTHR, 0, stream>>>((const u32*)d_in[0], (const u32*)d_in[1], (const u32*)d_in[2], (const u32*)d_in[3], (const u32*)d_in[4],
                                         (const u32*)d_in[6], (const u32*)d_in[7], (const u32*)d_in[8], (const u32*)d_in[9], (u32*)d_out, (unsigned char*)d_ws);
}
```

```cpp
#include <hip/hip_runtime.h>
#include <hip/hip_bf16.h>
#include <stdint.h>

typedef unsigned u32;
using u32x4 = __attribute__((ext_vector_type(4))) unsigned;
using u32x2 = __attribute__((ext_vector_type(2))) unsigned;
using f32x4 = __attribute__((ext_vector_type(4))) float;
using f32x2 = __attribute__((ext_vector_type(2))) float;
using bf16x2 = __attribute__((ext_vector_type(2))) __bf16;
using bf16x8 = __attribute__((ext_vector_type(8))) __bf16;
typedef __amdgpu_buffer_rsrc_t rsrc_t;
#define GAS __attribute__((address_space(1)))

#define RING 2
#define S0BAR 1
#define ROT_EXPR (48 * b)
#define AUX_K 19
#define AUX_V 19
#define AUX_W 2
#define WT_EARLY 1

constexpr int D = 640, H = 32, SK = 8192, DH = 128, F = 1728;
constexpr int NWG = 256, NTHR = 512;
constexpr int AUX_NT = 2, AUX_SC1 = 16;
constexpr int NSLOT = 512;
constexpr int OOB = (int)0x80000000;

constexpr unsigned WS_EPOCH = 0;
constexpr unsigned WS_QP = 4096, QP_STRIDE = 2048;
constexpr unsigned WS_FFN = WS_QP + 256 * QP_STRIDE, FFN_STRIDE = 2048;
constexpr unsigned WS_HOP1 = WS_FFN + 256 * FFN_STRIDE, HOP1_STRIDE = 1152;
constexpr unsigned WS_HOP2 = WS_HOP1 + 256 * HOP1_STRIDE, HOP2_STRIDE = 1280;
constexpr unsigned WS_END = WS_HOP2 + 256 * HOP2_STRIDE;

constexpr int L_QRED = 0;
constexpr int L_FRED = L_QRED + 2048;
constexpr int L_Q = L_FRED + 2048;
constexpr int L_PART = L_Q + 64;
constexpr int L_VV = L_PART + 8 * 132;
constexpr int L_RED = L_VV + 384;
constexpr int L_G = L_RED + 1280;
constexpr int L_CTR = L_G + 4;
constexpr int L_END = L_CTR + 4;

__device__ __forceinline__ float bflo(u32 u) { return __builtin_bit_cast(float, u << 16); }
__device__ __forceinline__ float bfhi(u32 u) { return __builtin_bit_cast(float, u & 0xffff0000u); }
__device__ __forceinline__ float rbf(float x) { return (float)(__bf16)x; }
__device__ __forceinline__ u32 pack_bf(float lo, float hi) {
  bf16x2 v = {(__bf16)lo, (__bf16)hi};
  return __builtin_bit_cast(u32, v);
}
__device__ __forceinline__ float dot2(u32 a, u32 b, float c) {
  return __builtin_amdgcn_fdot2_f32_bf16(__builtin_bit_cast(bf16x2, a), __builtin_bit_cast(bf16x2, b), c, false);
}
__device__ __forceinline__ f32x4 mfma(u32x4 a, u32x4 b, f32x4 c) {
  return __builtin_amdgcn_mfma_f32_16x16x32_bf16(__builtin_bit_cast(bf16x8, a), __builtin_bit_cast(bf16x8, b), c, 0, 0, 0);
}
template <int CTRL> __device__ __forceinline__ float dpp(float x) {
  return __builtin_bit_cast(float, __builtin_amdgcn_mov_dpp(__builtin_bit_cast(int, x), CTRL, 0xf, 0xf, true));
}
__device__ __forceinline__ float row16_sum(float x) {
  x += dpp<0xB1>(x);
  x += dpp<0x4E>(x);
  x += dpp<0x141>(x);
  x += dpp<0x140>(x);
  return x;
}
__device__ __forceinline__ float xrow_sum(float x) {
  auto s = __builtin_amdgcn_permlane16_swap(__float_as_uint(x), __float_as_uint(x), false, false);
  x = __uint_as_float(s[0]) + __uint_as_float(s[1]);
  auto t = __builtin_amdgcn_permlane32_swap(__float_as_uint(x), __float_as_uint(x), false, false);
  return __uint_as_float(t[0]) + __uint_as_float(t[1]);
}
__device__ __forceinline__ float xrow_max(float x) {
  auto s = __builtin_amdgcn_permlane16_swap(__float_as_uint(x), __float_as_uint(x), false, false);
  x = fmaxf(__uint_as_float(s[0]), __uint_as_float(s[1]));
  auto t = __builtin_amdgcn_permlane32_swap(__float_as_uint(x), __float_as_uint(x), false, false);
  return fmaxf(__uint_as_float(t[0]), __uint_as_float(t[1]));
}
#define LD_NT(r, voff, soff) __builtin_amdgcn_raw_buffer_load_b128((r), (voff), (soff), AUX_W)
#define LD_K(r, voff, soff) __builtin_amdgcn_raw_buffer_load_b128((r), (voff), (soff), AUX_K)
#define LD_V(r, voff, soff) __builtin_amdgcn_raw_buffer_load_b128((r), (voff), (soff), AUX_V)
#define LD_SC1(r, voff, soff) __builtin_amdgcn_raw_buffer_load_b128((r), (voff), (soff), AUX_SC1)
#define ST_SC1(v, r, voff, soff) __builtin_amdgcn_raw_buffer_store_b128((v), (r), (voff), (soff), AUX_SC1)
#define LDS_BARRIER() do { asm volatile("s_waitcnt lgkmcnt(0)" ::: "memory"); __builtin_amdgcn_s_barrier(); asm volatile("" ::: "memory"); } while (0)
__device__ __forceinline__ rsrc_t mk_rsrc(const void* p, unsigned bytes) { return __builtin_amdgcn_make_buffer_rsrc((void*)p, 0, bytes, 0x00020000); }
__device__ __forceinline__ u32x4 mk_unit(float a, float b, u32 tag) { u32x4 v = {__float_as_uint(a), tag, __float_as_uint(b), tag}; return v; }
__device__ __forceinline__ bool unit_ok(u32x4 v, u32 tag) { return v[1] == tag && v[3] == tag; }
__device__ __forceinline__ u32x4 onehot(u32 dup, const u32 (&msk)[4]) { u32x4 a = {dup & msk[0], dup & msk[1], dup & msk[2], dup & msk[3]}; return a; }

constexpr unsigned SPIN_CAP = 1u << 18;
#define STAMP(k) do { } while (0)
#define STAMP_DECL do { } while (0)
#define STAMP_FLUSH() do { } while (0)

__global__ void __launch_bounds__(NTHR, 2)
fused_decode(const u32* __restrict__ x, const u32* __restrict__ kc, const u32* __restrict__ vc, const u32* __restrict__ wq,
             const u32* __restrict__ wo, const u32* __restrict__ wg, const u32* __restrict__ wu, const u32* __restrict__ wd,
             const u32* __restrict__ gw, u32* __restrict__ out, unsigned char* __restrict__ ws) {
  __shared__ __attribute__((aligned(16))) float lds[L_END];
  u32* const ldsu = (u32*)lds;
  const int tid = threadIdx.x, lane = tid & 63, w = __builtin_amdgcn_readfirstlane(tid >> 6);
  const int r4 = lane >> 4, c = lane & 15;
  const int blk = blockIdx.x, xg = blk & 7, li = blk >> 3;
  const int h = xg * 4 + (li >> 3), j = li & 7, b = j >> 2, s = j & 3;
  const int fb = (h & 3) * 8 + (h >> 2);
  const bool has_ffn = fb < 27;
  const int bh = b * 32 + h;
  STAMP_DECL;
  STAMP(0);

  const rsrc_t rws = mk_rsrc(ws, WS_END);
  const rsrc_t rk = mk_rsrc((const unsigned char*)kc + (size_t)bh * (SK * DH * 2), SK * DH * 2);
  const rsrc_t rv = mk_rsrc((const unsigned char*)vc + (size_t)bh * (SK * DH * 2), SK * DH * 2);

  u32 epoch = __builtin_amdgcn_raw_buffer_load_b32(rws, 0, WS_EPOCH, AUX_SC1);
  u32 xs[3];
  u32x4 qw[3], fw[3];
  {
    const rsrc_t rx = mk_rsrc(x, 2 * D * 2);
    const rsrc_t rwq = mk_rsrc(wq, D * 4096 * 2);
    const int row0 = 80 * j + 10 * w + r4;
#pragma unroll
    for (int u = 0; u < 3; u++) {
      const bool ok = (u < 2) || (r4 < 2);
      xs[u] = __builtin_amdgcn_raw_buffer_load_b16(rx, ok ? ((c >> 3) * D + row0 + 4 * u) * 2 : OOB, 0, AUX_W);
    }
#pragma unroll
    for (int u = 0; u < 3; u++) {
      const bool ok = (u < 2) || (r4 < 2);
      qw[u] = LD_NT(rwq, ok ? ((row0 + 4 * u) * 4096 + 128 * h + 8 * c) * 2 : OOB, 0);
    }
    __builtin_amdgcn_sched_barrier(0); __builtin_amdgcn_s_barrier(); __builtin_amdgcn_sched_barrier(0);
    const int fbc = has_ffn ? fb : 0;
    const GAS unsigned char* fbase = (const GAS unsigned char*)((c < 8) ? wg : wu) + (64 * fbc + 8 * (c & 7)) * 2;
#pragma unroll
    for (int u = 0; u < 3; u++) {
      const int row = (u < 2 || r4 < 2) ? row0 + 4 * u : row0;
      fw[u] = AUX_W == 2 ? __builtin_nontemporal_load((const GAS u32x4*)(fbase + (size_t)row * (F * 2))) : *(const GAS u32x4*)(fbase + (size_t)row * (F * 2));
    }
  }
  __builtin_amdgcn_sched_barrier(0);
  u32 msk[4];
#pragma unroll
  for (int k = 0; k < 4; k++) msk[k] = (k == ((lane & 7) >> 1)) ? ((lane & 1) ? 0xffff0000u : 0x0000ffffu) : 0u;
  if (tid == 0) ldsu[L_CTR] = 8 * RING;
  u32x4 xa[3];
  {
    f32x4 qa = {0, 0, 0, 0};
#pragma unroll
    for (int u = 0; u < 3; u++) { xa[u] = onehot(xs[u] * 0x10001u, msk); qa = mfma(xa[u], qw[u], qa); }
    *(f32x4*)(lds + L_QRED + w * 256 + (lane >> 5) * 128 + 8 * c + 4 * (r4 & 1)) = qa;
  }
  STAMP(1);
  LDS_BARRIER();
  STAMP(2);
  const u32 tag = __builtin_amdgcn_readfirstlane(epoch) + 1u;
  if (w < 2) {
    f32x2 sum = {0, 0};
#pragma unroll
    for (int ww = 0; ww < 8; ww++) sum += *(const f32x2*)(lds + L_QRED + ww * 256 + tid * 2);
    ST_SC1(mk_unit(sum[0], sum[1], tag), rws, tid * 16, WS_QP + (h * 8 + j) * QP_STRIDE);
  }
  u32x4 wt[6];
  const int wtoff = c < 10 ? (r4 * D + 8 * c) * 2 : OOB;
#define LOAD_WT() do { \
    const rsrc_t rwo = mk_rsrc(wo, 4096 * D * 2), rwd = mk_rsrc(wd, has_ffn ? F * D * 2 : 0); \
    _Pragma("unroll") for (int i = 0; i < 4; i++) wt[i] = LD_NT(rwo, wtoff, ((128 * h + 16 * w + 4 * i) * D + 80 * j) * 2); \
    _Pragma("unroll") for (int i = 0; i < 2; i++) wt[4 + i] = LD_NT(rwd, wtoff, ((64 * fb + 8 * w + 4 * i) * D + 80 * j) * 2); } while (0)
  if (WT_EARLY) LOAD_WT();
  u32x4 gx0, gx1, gg0, gg1;
  if (w >= 6) {
    const rsrc_t rxb = mk_rsrc((const unsigned char*)x + (w - 6) * D * 2, D * 2), rgw = mk_rsrc(gw, D * 2);
    gx0 = LD_NT(rxb, lane * 16, 0); gx1 = LD_NT(rxb, lane * 16 + 1024, 0);
    gg0 = LD_NT(rgw, lane * 16, 0); gg1 = LD_NT(rgw, lane * 16 + 1024, 0);
  }
  u32x4 qg[8];
  if (w == 0) {
#pragma unroll
    for (int p = 0; p < 8; p++) qg[p] = LD_SC1(rws, lane * 16 + b * 1024, WS_QP + (h * 8 + p) * QP_STRIDE);
  }
  const int kvoff = lane * 16;
  const int kvbase = s * 16384;
  u32x4 kr[RING], vr[RING];
  const int rot = ROT_EXPR;
#define SLOT_OFF(t) (kvbase + ((((t) + rot) & 511) >> 4) * 65536 + ((t) & 15) * 1024)
#pragma unroll
  for (int i = 0; i < RING - 1; i++) { kr[i] = LD_K(rk, kvoff, SLOT_OFF(w + 8 * i)); vr[i] = LD_V(rv, kvoff, SLOT_OFF(w + 8 * i)); }
  __builtin_amdgcn_sched_barrier(0);
  STAMP(3);
  {
    f32x4 fa = {0, 0, 0, 0};
#pragma unroll
    for (int u = 0; u < 3; u++) fa = mfma(xa[u], fw[u], fa);
    *(f32x4*)(lds + L_FRED + w * 256 + (lane >> 5) * 128 + 8 * c + 4 * (r4 & 1)) = fa;
  }
  if (w >= 6) {
    float gs = 0.f;
#pragma unroll
    for (int k = 0; k < 4; k++) {
      gs += rbf(bflo(gx0[k]) * bflo(gg0[k])) + rbf(bfhi(gx0[k]) * bfhi(gg0[k]));
      gs += rbf(bflo(gx1[k]) * bflo(gg1[k])) + rbf(bfhi(gx1[k]) * bfhi(gg1[k]));
    }
    gs = xrow_sum(row16_sum(gs));
    const float g = rbf(1.f / (1.f + __expf(-rbf(gs))));
    if (lane == 0) { lds[L_G + w - 6] = g; lds[L_G + 2 + w - 6] = rbf(1.f - g); }
  }
  if (w == 0) {
    unsigned spins = 0;
    while (true) {
      bool ok = true;
#pragma unroll
      for (int p = 0; p < 8; p++) ok = ok && unit_ok(qg[p], tag);
      if (__all(ok) || ++spins > SPIN_CAP) break;
      __builtin_amdgcn_s_sleep(1);
      int vo = lane * 16 + b * 1024;
      asm volatile("" : "+v"(vo) :: "memory");
#pragma unroll
      for (int p = 0; p < 8; p++) qg[p] = LD_SC1(rws, vo, WS_QP + (h * 8 + p) * QP_STRIDE);
    }
    float q0 = 0.f, q1 = 0.f;
#pragma unroll
    for (int p = 0; p < 8; p++) { q0 += __uint_as_float(qg[p][0]); q1 += __uint_as_float(qg[p][2]); }
    ldsu[L_Q + lane] = pack_bf(q0, q1);
  }
  STAMP(4);
  LDS_BARRIER();
  const u32x4 qp = *(const u32x4*)(ldsu + L_Q + c * 4);
  asm volatile("" :: "v"(qp));
  if (w == 2 || w == 3) {
    float g = 0.f, u = 0.f;
#pragma unroll
    for (int ww = 0; ww < 8; ww++) { g += lds[L_FRED + ww * 256 + (w - 2) * 128 + lane]; u += lds[L_FRED + ww * 256 + (w - 2) * 128 + 64 + lane]; }
    if (has_ffn) ST_SC1(mk_unit(g, u, tag), rws, (tid - 128) * 16, WS_FFN + (h * 8 + j) * FFN_STRIDE);
  }
  STAMP(5);

  constexpr float C2 = 0.08838834764831845f * 1.4426950408889634f;
  float mraw = -INFINITY, mc = -INFINITY, l = 0.f;
  f32x4 acc = {0, 0, 0, 0};
  int tn = w + 8 * (RING - 1);
  int tq[RING];
#pragma unroll
  for (int i = 0; i < RING; i++) tq[i] = 0;
  const u32 ctr_addr = (u32)(L_CTR * 4), one = 1u;
  for (;;) {
#pragma unroll
    for (int i = 0; i < RING; i++) {
      u32 pf; unsigned long long ex;
      asm volatile("s_mov_b64 %1, exec\n\ts_mov_b64 exec, 1\n\tds_add_rtn_u32 %0, %2, %3\n\ts_mov_b64 exec, %1" : "=&v"(pf), "=&s"(ex) : "v"(ctr_addr), "v"(one) : "memory");
      const int e = (i + RING - 1) % RING;
      const int so = tn < NSLOT ? SLOT_OFF(tn) : OOB;
      kr[e] = LD_K(rk, kvoff, so); vr[e] = LD_V(rv, kvoff, so);
      tq[e] = tn;
      __builtin_amdgcn_sched_barrier(0);
      if (tq[i] >= NSLOT) { asm volatile("s_waitcnt lgkmcnt(0)" :: "v"(pf) : "memory"); goto loop_done; }
      float sc = dot2(kr[i][0], qp[0], 0.f); sc = dot2(kr[i][1], qp[1], sc); sc = dot2(kr[i][2], qp[2], sc); sc = dot2(kr[i][3], qp[3], sc);
      sc = row16_sum(sc);
      if (__builtin_amdgcn_ballot_w64(sc > mraw) != 0) {
        const float mn = xrow_max(fmaxf(sc, mraw));
        const float mnc = mn * C2;
        const float al = __builtin_amdgcn_exp2f(mc - mnc);
        acc *= al; l *= al; mraw = mn; mc = mnc;
      }
      const float p = __builtin_amdgcn_exp2f(fmaf(sc, C2, -mc));
      l += p;
      acc = mfma(onehot(pack_bf(p, p), msk), vr[i], acc);
      asm volatile("" : "+v"(acc), "+v"(l));
      asm volatile("s_waitcnt lgkmcnt(0)\n\tv_readfirstlane_b32 %0, %1" : "=s"(tn) : "v"(pf) : "memory");
      __builtin_amdgcn_sched_barrier(0);
    }
  }
loop_done:
  STAMP(6);
  if (!WT_EARLY) LOAD_WT();

  l = xrow_sum(l);
  if (lane < 32) *(f32x4*)(lds + L_PART + w * 132 + 8 * c + 4 * r4) = acc;
  if (lane == 0) { lds[L_PART + w * 132 + 128] = mc; lds[L_PART + w * 132 + 129] = l; }
  LDS_BARRIER();
  STAMP(7);
  if (w == 0) {
    float mw[8], M = -INFINITY;
#pragma unroll
    for (int ww = 0; ww < 8; ww++) { mw[ww] = lds[L_PART + ww * 132 + 128]; M = fmaxf(M, mw[ww]); }
    float L = 0.f, o0 = 0.f, o1 = 0.f;
#pragma unroll
    for (int ww = 0; ww < 8; ww++) {
      const float e = __builtin_amdgcn_exp2f(mw[ww] - M);
      const f32x2 a = *(const f32x2*)(lds + L_PART + ww * 132 + lane * 2);
      L = fmaf(e, lds[L_PART + ww * 132 + 129], L); o0 = fmaf(e, a[0], o0); o1 = fmaf(e, a[1], o1);
    }
    const int so = WS_HOP1 + (h * 8 + j) * HOP1_STRIDE;
    ST_SC1(mk_unit(o0, o1, tag), rws, lane * 16, so);
    if (lane == 0) ST_SC1(mk_unit(M, L, tag), rws, 1024, so);
  } else if (w <= 2) {
    const int bb = w - 1;
    u32x4 a[4], ml[4];
    unsigned spins = 0;
    while (true) {
      int vo = lane * 16, vz = 1024;
      asm volatile("" : "+v"(vo), "+v"(vz) :: "memory");
      bool ok = true;
#pragma unroll
      for (int p = 0; p < 4; p++) { const int so = WS_HOP1 + (h * 8 + bb * 4 + p) * HOP1_STRIDE; a[p] = LD_SC1(rws, vo, so); ml[p] = LD_SC1(rws, vz, so); }
#pragma unroll
      for (int p = 0; p < 4; p++) ok = ok && unit_ok(a[p], tag) && unit_ok(ml[p], tag);
      if (__all(ok) || ++spins > SPIN_CAP) break;
      __builtin_amdgcn_s_sleep(1);
    }
    float M = -INFINITY;
#pragma unroll
    for (int p = 0; p < 4; p++) M = fmaxf(M, __uint_as_float(ml[p][0]));
    float L = 0.f, o0 = 0.f, o1 = 0.f;
#pragma unroll
    for (int p = 0; p < 4; p++) {
      const float e = __builtin_amdgcn_exp2f(__uint_as_float(ml[p][0]) - M);
      L = fmaf(e, __uint_as_float(ml[p][2]), L); o0 = fmaf(e, __uint_as_float(a[p][0]), o0); o1 = fmaf(e, __uint_as_float(a[p][2]), o1);
    }
    const float inv = 1.f / L;
    o0 *= inv; o1 *= inv;
    *(u32x2*)(ldsu + L_VV + bb * 192 + lane * 2) = u32x2{pack_bf(o0, o0), pack_bf(o1, o1)};
  } else if (w <= 4) {
    const int bb = w - 3;
    float g = 0.f, u = 0.f;
    if (has_ffn) {
      u32x4 a[8];
      unsigned spins = 0;
      while (true) {
        int vo = lane * 16 + bb * 1024;
        asm volatile("" : "+v"(vo) :: "memory");
        bool ok = true;
#pragma unroll
        for (int p = 0; p < 8; p++) a[p] = LD_SC1(rws, vo, WS_FFN + (h * 8 + p) * FFN_STRIDE);
#pragma unroll
        for (int p = 0; p < 8; p++) ok = ok && unit_ok(a[p], tag);
        if (__all(ok) || ++spins > SPIN_CAP) break;
        __builtin_amdgcn_s_sleep(1);
      }
#pragma unroll
      for (int p = 0; p < 8; p++) { g += __uint_as_float(a[p][0]); u += __uint_as_float(a[p][2]); }
    }
    g = rbf(g); u = rbf(u);
    const float t = 0.7978845608028654f * (g + 0.044715f * g * g * g);
    const float th = 1.f - 2.f / (1.f + __expf(2.f * t));
    const float hid = rbf(0.5f * g * (1.f + th)) * u;
    ldsu[L_VV + bb * 192 + 128 + lane] = pack_bf(hid, hid);
  }
  LDS_BARRIER();
  STAMP(8);
  {
    f32x4 ao = {0, 0, 0, 0}, ad = {0, 0, 0, 0};
    const u32* vv = ldsu + L_VV + (c >> 3) * 192;
#pragma unroll
    for (int i = 0; i < 4; i++) ao = mfma(onehot(vv[16 * w + 4 * i + r4], msk), wt[i], ao);
#pragma unroll
    for (int i = 0; i < 2; i++) ad = mfma(onehot(vv[128 + 8 * w + 4 * i + r4], msk), wt[4 + i], ad);
    const int bb = lane >> 5;
    const float g = lds[L_G + bb], g1 = lds[L_G + 2 + bb];
    if (c < 10) *(f32x4*)(lds + L_RED + w * 160 + bb * 80 + 8 * c + 4 * (r4 & 1)) = g * ao + g1 * ad;
  }
  LDS_BARRIER();
  STAMP(9);
  if (tid < 80) {
    f32x2 sum = {0, 0};
#pragma unroll
    for (int ww = 0; ww < 8; ww++) sum += *(const f32x2*)(lds + L_RED + ww * 160 + tid * 2);
    ST_SC1(mk_unit(sum[0], sum[1], tag), rws, tid * 16, WS_HOP2 + (j * 32 + h) * HOP2_STRIDE);
  }
  if (w == 0 && h < 20) {
    const int bb = h / 10, cg = h % 10;
    const int hp = lane >> 1, half = lane & 1;
    const int vo = hp * HOP2_STRIDE + (bb * 40 + cg * 4 + half * 2) * 16;
    const int so = WS_HOP2 + j * 32 * HOP2_STRIDE;
    u32x4 a0, a1;
    unsigned spins = 0;
    while (true) {
      int vv = vo;
      asm volatile("" : "+v"(vv) :: "memory");
      a0 = LD_SC1(rws, vv, so);
      a1 = LD_SC1(rws, vv + 16, so);
      if (__all(unit_ok(a0, tag) && unit_ok(a1, tag)) || ++spins > SPIN_CAP) break;
      __builtin_amdgcn_s_sleep(1);
    }
    float v[4] = {__uint_as_float(a0[0]), __uint_as_float(a0[2]), __uint_as_float(a1[0]), __uint_as_float(a1[2])};
#pragma unroll
    for (int k = 0; k < 4; k++) {
      v[k] += dpp<0x4E>(v[k]);
      v[k] += dpp<0x124>(v[k]);
      v[k] += dpp<0x128>(v[k]);
      v[k] = xrow_sum(v[k]);
    }
    if (lane < 2) {
      u32x2 r = {pack_bf(v[0], v[1]), pack_bf(v[2], v[3])};
      *(u32x2*)(out + (bb * 640 + 80 * j + 8 * cg + 4 * half) / 2) = r;
    }
    if (blk == 0 && lane == 0) __builtin_amdgcn_raw_buffer_store_b32(tag, rws, 0, WS_EPOCH, AUX_SC1);
  }
  STAMP(10);
  STAMP_FLUSH();
}

extern "C" void kernel_launch(void* const* d_in, const int* in_sizes, int n_in, void* d_out, int out_size, void* d_ws, size_t ws_size, hipStream_t stream) {
  fused_decode<<<NWG, NTHR, 0, stream>>>((const u32*)d_in[0], (const u32*)d_in[1], (const u32*)d_in[2], (const u32*)d_in[3], (const u32*)d_in[4],
                                         (const u32*)d_in[6], (const u32*)d_in[7], (const u32*)d_in[8], (const u32*)d_in[9], (u32*)d_out, (unsigned char*)d_ws);
}
```

```cpp
#include <hip/hip_runtime.h>
#include <hip/hip_bf16.h>
#include <stdint.h>

typedef unsigned u32;
using u32x4 = __attribute__((ext_vector_type(4))) unsigned;
using u32x2 = __attribute__((ext_vector_type(2))) unsigned;
using f32x4 = __attribute__((ext_vector_type(4))) float;
using f32x2 = __attribute__((ext_vector_type(2))) float;
using bf16x2 = __attribute__((ext_vector_type(2))) __bf16;
using bf16x8 = __attribute__((ext_vector_type(8))) __bf16;
typedef __amdgpu_buffer_rsrc_t rsrc_t;
#define GAS __attribute__((address_space(1)))

#define RING 2
#define S0BAR 1
#define ROT_EXPR (48 * b)
#define AUX_K 2
#define AUX_V 2
#define AUX_W 2
#define WT_EARLY 1

constexpr int D = 640, H = 32, SK = 8192, DH = 128, F = 1728;
constexpr int NWG = 256, NTHR = 512;
constexpr int AUX_NT = 2, AUX_SC1 = 16;
constexpr int NSLOT = 512;
constexpr int OOB = (int)0x80000000;

constexpr unsigned WS_EPOCH = 0;
constexpr unsigned WS_QP = 4096, QP_STRIDE = 2048;
constexpr unsigned WS_FFN = WS_QP + 256 * QP_STRIDE, FFN_STRIDE = 2048;
constexpr unsigned WS_HOP1 = WS_FFN + 256 * FFN_STRIDE, HOP1_STRIDE = 1152;
constexpr unsigned WS_HOP2 = WS_HOP1 + 256 * HOP1_STRIDE, HOP2_STRIDE = 1280;
constexpr unsigned WS_END = WS_HOP2 + 256 * HOP2_STRIDE;

constexpr int L_QRED = 0;
constexpr int L_FRED = L_QRED + 2048;
constexpr int L_Q = L_FRED + 2048;
constexpr int L_PART = L_Q + 64;
constexpr int L_VV = L_PART + 8 * 132;
constexpr int L_RED = L_VV + 384;
constexpr int L_G = L_RED + 1280;
constexpr int L_CTR = L_G + 4;
constexpr int L_END = L_CTR + 4;

__device__ __forceinline__ float bflo(u32 u) { return __builtin_bit_cast(float, u << 16); }
__device__ __forceinline__ float bfhi(u32 u) { return __builtin_bit_cast(float, u & 0xffff0000u); }
__device__ __forceinline__ float rbf(float x) { return (float)(__bf16)x; }
__device__ __forceinline__ u32 pack_bf(float lo, float hi) {
  bf16x2 v = {(__bf16)lo, (__bf16)hi};
  return __builtin_bit_cast(u32, v);
}
__device__ __forceinline__ float dot2(u32 a, u32 b, float c) {
  return __builtin_amdgcn_fdot2_f32_bf16(__builtin_bit_cast(bf16x2, a), __builtin_bit_cast(bf16x2, b), c, false);
}
__device__ __forceinline__ f32x4 mfma(u32x4 a, u32x4 b, f32x4 c) {
  return __builtin_amdgcn_mfma_f32_16x16x32_bf16(__builtin_bit_cast(bf16x8, a), __builtin_bit_cast(bf16x8, b), c, 0, 0, 0);
}
template <int CTRL> __device__ __forceinline__ float dpp(float x) {
  return __builtin_bit_cast(float, __builtin_amdgcn_mov_dpp(__builtin_bit_cast(int, x), CTRL, 0xf, 0xf, true));
}
__device__ __forceinline__ float row16_sum(float x) {
  x += dpp<0xB1>(x);
  x += dpp<0x4E>(x);
  x += dpp<0x141>(x);
  x += dpp<0x140>(x);
  return x;
}
__device__ __forceinline__ float xrow_sum(float x) {
  auto s = __builtin_amdgcn_permlane16_swap(__float_as_uint(x), __float_as_uint(x), false, false);
  x = __uint_as_float(s[0]) + __uint_as_float(s[1]);
  auto t = __builtin_amdgcn_permlane32_swap(__float_as_uint(x), __float_as_uint(x), false, false);
  return __uint_as_float(t[0]) + __uint_as_float(t[1]);
}
__device__ __forceinline__ float xrow_max(float x) {
  auto s = __builtin_amdgcn_permlane16_swap(__float_as_uint(x), __float_as_uint(x), false, false);
  x = fmaxf(__uint_as_float(s[0]), __uint_as_float(s[1]));
  auto t = __builtin_amdgcn_permlane32_swap(__float_as_uint(x), __float_as_uint(x), false, false);
  return fmaxf(__uint_as_float(t[0]), __uint_as_float(t[1]));
}
#define LD_NT(r, voff, soff) __builtin_amdgcn_raw_buffer_load_b128((r), (voff), (soff), AUX_W)
#define LD_K(r, voff, soff) __builtin_amdgcn_raw_buffer_load_b128((r), (voff), (soff), AUX_K)
#define LD_V(r, voff, soff) __builtin_amdgcn_raw_buffer_load_b128((r), (voff), (soff), AUX_V)
#define LD_SC1(r, voff, soff) __builtin_amdgcn_raw_buffer_load_b128((r), (voff), (soff), AUX_SC1)
#define ST_SC1(v, r, voff, soff) __builtin_amdgcn_raw_buffer_store_b128((v), (r), (voff), (soff), AUX_SC1)
#define LDS_BARRIER() do { asm volatile("s_waitcnt lgkmcnt(0)" ::: "memory"); __builtin_amdgcn_s_barrier(); asm volatile("" ::: "memory"); } while (0)
__device__ __forceinline__ rsrc_t mk_rsrc(const void* p, unsigned bytes) { return __builtin_amdgcn_make_buffer_rsrc((void*)p, 0, bytes, 0x00020000); }
__device__ __forceinline__ u32x4 mk_unit(float a, float b, u32 tag) { u32x4 v = {__float_as_uint(a), tag, __float_as_uint(b), tag}; return v; }
__device__ __forceinline__ bool unit_ok(u32x4 v, u32 tag) { return v[1] == tag && v[3] == tag; }
__device__ __forceinline__ u32x4 onehot(u32 dup, const u32 (&msk)[4]) { u32x4 a = {dup & msk[0], dup & msk[1], dup & msk[2], dup & msk[3]}; return a; }

constexpr unsigned SPIN_CAP = 1u << 18;
#define STAMP(k) do { } while (0)
#define STAMP_DECL do { } while (0)
#define STAMP_FLUSH() do { } while (0)

__global__ void __launch_bounds__(NTHR, 2)
fused_decode(const u32* __restrict__ x, const u32* __restrict__ kc, const u32* __restrict__ vc, const u32* __restrict__ wq,
             const u32* __restrict__ wo, const u32* __restrict__ wg, const u32* __restrict__ wu, const u32* __restrict__ wd,
             const u32* __restrict__ gw, u32* __restrict__ out, unsigned char* __restrict__ ws) {
  __shared__ __attribute__((aligned(16))) float lds[L_END];
  u32* const ldsu = (u32*)lds;
  const int tid = threadIdx.x, lane = tid & 63, w = __builtin_amdgcn_readfirstlane(tid >> 6);
  const int r4 = lane >> 4, c = lane & 15;
  const int blk = blockIdx.x, xg = blk & 7, li = blk >> 3;
  const int h = xg * 4 + (li >> 3), j = li & 7, b = j >> 2, s = j & 3;
  const int fb = (h & 3) * 8 + (h >> 2);
  const bool has_ffn = fb < 27;
  const int bh = b * 32 + h;
  STAMP_DECL;
  STAMP(0);

  const rsrc_t rws = mk_rsrc(ws, WS_END);
  const rsrc_t rk = mk_rsrc((const unsigned char*)kc + (size_t)bh * (SK * DH * 2), SK * DH * 2);
  const rsrc_t rv = mk_rsrc((const unsigned char*)vc + (size_t)bh * (SK * DH * 2), SK * DH * 2);

  u32 epoch = __builtin_amdgcn_raw_buffer_load_b32(rws, 0, WS_EPOCH, AUX_SC1);
  u32 xs[3];
  u32x4 qw[3], fw[3];
  {
    const rsrc_t rx = mk_rsrc(x, 2 * D * 2);
    const rsrc_t rwq = mk_rsrc(wq, D * 4096 * 2);
    const int row0 = 80 * j + 10 * w + r4;
#pragma unroll
    for (int u = 0; u < 3; u++) {
      const bool ok = (u < 2) || (r4 < 2);
      xs[u] = __builtin_amdgcn_raw_buffer_load_b16(rx, ok ? ((c >> 3) * D + row0 + 4 * u) * 2 : OOB, 0, AUX_W);
    }
#pragma unroll
    for (int u = 0; u < 3; u++) {
      const bool ok = (u < 2) || (r4 < 2);
      qw[u] = LD_NT(rwq, ok ? ((row0 + 4 * u) * 4096 + 128 * h + 8 * c) * 2 : OOB, 0);
    }
    __builtin_amdgcn_sched_barrier(0); __builtin_amdgcn_s_barrier(); __builtin_amdgcn_sched_barrier(0);
    const int fbc = has_ffn ? fb : 0;
    const GAS unsigned char* fbase = (const GAS unsigned char*)((c < 8) ? wg : wu) + (64 * fbc + 8 * (c & 7)) * 2;
#pragma unroll
    for (int u = 0; u < 3; u++) {
      const int row = (u < 2 || r4 < 2) ? row0 + 4 * u : row0;
      fw[u] = AUX_W == 2 ? __builtin_nontemporal_load((const GAS u32x4*)(fbase + (size_t)row * (F * 2))) : *(const GAS u32x4*)(fbase + (size_t)row * (F * 2));
    }
  }
  __builtin_amdgcn_sched_barrier(0);
  u32 msk[4];
#pragma unroll
  for (int k = 0; k < 4; k++) msk[k] = (k == ((lane & 7) >> 1)) ? ((lane & 1) ? 0xffff0000u : 0x0000ffffu) : 0u;
  if (tid == 0) ldsu[L_CTR] = 8 * RING;
  u32x4 xa[3];
  {
    f32x4 qa = {0, 0, 0, 0};
#pragma unroll
    for (int u = 0; u < 3; u++) { xa[u] = onehot(xs[u] * 0x10001u, msk); qa = mfma(xa[u], qw[u], qa); }
    *(f32x4*)(lds + L_QRED + w * 256 + (lane >> 5) * 128 + 8 * c + 4 * (r4 & 1)) = qa;
  }
  STAMP(1);
  LDS_BARRIER();
  STAMP(2);
  const u32 tag = __builtin_amdgcn_readfirstlane(epoch) + 1u;
  if (w < 2) {
    f32x2 sum = {0, 0};
#pragma unroll
    for (int ww = 0; ww < 8; ww++) sum += *(const f32x2*)(lds + L_QRED + ww * 256 + tid * 2);
    ST_SC1(mk_unit(sum[0], sum[1], tag), rws, tid * 16, WS_QP + (h * 8 + j) * QP_STRIDE);
  }
  u32x4 wt[6];
  const int wtoff = c < 10 ? (r4 * D + 8 * c) * 2 : OOB;
#define LOAD_WT() do { \
    const rsrc_t rwo = mk_rsrc(wo, 4096 * D * 2), rwd = mk_rsrc(wd, has_ffn ? F * D * 2 : 0); \
    _Pragma("unroll") for (int i = 0; i < 4; i++) wt[i] = LD_NT(rwo, wtoff, ((128 * h + 16 * w + 4 * i) * D + 80 * j) * 2); \
    _Pragma("unroll") for (int i = 0; i < 2; i++) wt[4 + i] = LD_NT(rwd, wtoff, ((64 * fb + 8 * w + 4 * i) * D + 80 * j) * 2); } while (0)
  if (WT_EARLY) LOAD_WT();
  u32x4 gx0, gx1, gg0, gg1;
  if (w >= 6) {
    const rsrc_t rxb = mk_rsrc((const unsigned char*)x + (w - 6) * D * 2, D * 2), rgw = mk_rsrc(gw, D * 2);
    gx0 = LD_NT(rxb, lane * 16, 0); gx1 = LD_NT(rxb, lane * 16 + 1024, 0);
    gg0 = LD_NT(rgw, lane * 16, 0); gg1 = LD_NT(rgw, lane * 16 + 1024, 0);
  }
  u32x4 qg[8];
  if (w == 0) {
#pragma unroll
    for (int p = 0; p < 8; p++) qg[p] = LD_SC1(rws, lane * 16 + b * 1024, WS_QP + (h * 8 + p) * QP_STRIDE);
  }
  const int kvoff = lane * 16;
  const int kvbase = s * 16384;
  u32x4 kr[RING], vr[RING];
  const int rot = ROT_EXPR;
#define SLOT_OFF(t) (kvbase + ((((t) + rot) & 511) >> 4) * 65536 + ((t) & 15) * 1024)
#pragma unroll
  for (int i = 0; i < RING - 1; i++) { kr[i] = LD_K(rk, kvoff, SLOT_OFF(w + 8 * i)); vr[i] = LD_V(rv, kvoff, SLOT_OFF(w + 8 * i)); }
  __builtin_amdgcn_sched_barrier(0);
  STAMP(3);
  {
    f32x4 fa = {0, 0, 0, 0};
#pragma unroll
    for (int u = 0; u < 3; u++) fa = mfma(xa[u], fw[u], fa);
    *(f32x4*)(lds + L_FRED + w * 256 + (lane >> 5) * 128 + 8 * c + 4 * (r4 & 1)) = fa;
  }
  if (w >= 6) {
    float gs = 0.f;
#pragma unroll
    for (int k = 0; k < 4; k++) {
      gs += rbf(bflo(gx0[k]) * bflo(gg0[k])) + rbf(bfhi(gx0[k]) * bfhi(gg0[k]));
      gs += rbf(bflo(gx1[k]) * bflo(gg1[k])) + rbf(bfhi(gx1[k]) * bfhi(gg1[k]));
    }
    gs = xrow_sum(row16_sum(gs));
    const float g = rbf(1.f / (1.f + __expf(-rbf(gs))));
    if (lane == 0) { lds[L_G + w - 6] = g; lds[L_G + 2 + w - 6] = rbf(1.f - g); }
  }
  if (w == 0) {
    unsigned spins = 0;
    while (true) {
      bool ok = true;
#pragma unroll
      for (int p = 0; p < 8; p++) ok = ok && unit_ok(qg[p], tag);
      if (__all(ok) || ++spins > SPIN_CAP) break;
      __builtin_amdgcn_s_sleep(1);
      int vo = lane * 16 + b * 1024;
      asm volatile("" : "+v"(vo) :: "memory");
#pragma unroll
      for (int p = 0; p < 8; p++) qg[p] = LD_SC1(rws, vo, WS_QP + (h * 8 + p) * QP_STRIDE);
    }
    float q0 = 0.f, q1 = 0.f;
#pragma unroll
    for (int p = 0; p < 8; p++) { q0 += __uint_as_float(qg[p][0]); q1 += __uint_as_float(qg[p][2]); }
    ldsu[L_Q + lane] = pack_bf(q0, q1);
  }
  STAMP(4);
  LDS_BARRIER();
  const u32x4 qp = *(const u32x4*)(ldsu + L_Q + c * 4);
  asm volatile("" :: "v"(qp));
  if (w == 2 || w == 3) {
    float g = 0.f, u = 0.f;
#pragma unroll
    for (int ww = 0; ww < 8; ww++) { g += lds[L_FRED + ww * 256 + (w - 2) * 128 + lane]; u += lds[L_FRED + ww * 256 + (w - 2) * 128 + 64 + lane]; }
    if (has_ffn) ST_SC1(mk_unit(g, u, tag), rws, (tid - 128) * 16, WS_FFN + (h * 8 + j) * FFN_STRIDE);
  }
  STAMP(5);

  constexpr float C2 = 0.08838834764831845f * 1.4426950408889634f;
  float mraw = -INFINITY, mc = -INFINITY, l = 0.f;
  f32x4 acc = {0, 0, 0, 0};
  int tn = w + 8 * (RING - 1);
  int tq[RING];
#pragma unroll
  for (int i = 0; i < RING; i++) tq[i] = 0;
  const u32 ctr_addr = (u32)(L_CTR * 4), one = 1u;
  for (;;) {
#pragma unroll
    for (int i = 0; i < RING; i++) {
      u32 pf; unsigned long long ex;
      asm volatile("s_mov_b64 %1, exec\n\ts_mov_b64 exec, 1\n\tds_add_rtn_u32 %0, %2, %3\n\ts_mov_b64 exec, %1" : "=&v"(pf), "=&s"(ex) : "v"(ctr_addr), "v"(one) : "memory");
      const int e = (i + RING - 1) % RING;
      const int so = tn < NSLOT ? SLOT_OFF(tn) : OOB;
      kr[e] = LD_K(rk, kvoff, so); vr[e] = LD_V(rv, kvoff, so);
      tq[e] = tn;
      __builtin_amdgcn_sched_barrier(0);
      if (tq[i] >= NSLOT) { asm volatile("s_waitcnt lgkmcnt(0)" :: "v"(pf) : "memory"); goto loop_done; }
      float sc = dot2(kr[i][0], qp[0], 0.f); sc = dot2(kr[i][1], qp[1], sc); sc = dot2(kr[i][2], qp[2], sc); sc = dot2(kr[i][3], qp[3], sc);
      sc = row16_sum(sc);
      if (__builtin_amdgcn_ballot_w64(sc > mraw) != 0) {
        const float mn = xrow_max(fmaxf(sc, mraw));
        const float mnc = mn * C2;
        const float al = __builtin_amdgcn_exp2f(mc - mnc);
        acc *= al; l *= al; mraw = mn; mc = mnc;
      }
      const float p = __builtin_amdgcn_exp2f(fmaf(sc, C2, -mc));
      l += p;
      acc = mfma(onehot(pack_bf(p, p), msk), vr[i], acc);
      asm volatile("" : "+v"(acc), "+v"(l));
      asm volatile("s_waitcnt lgkmcnt(0)\n\tv_readfirstlane_b32 %0, %1" : "=s"(tn) : "v"(pf) : "memory");
      __builtin_amdgcn_sched_barrier(0);
    }
  }
loop_done:
  STAMP(6);
  if (!WT_EARLY) LOAD_WT();

  l = xrow_sum(l);
  if (lane < 32) *(f32x4*)(lds + L_PART + w * 132 + 8 * c + 4 * r4) = acc;
  if (lane == 0) { lds[L_PART + w * 132 + 128] = mc; lds[L_PART + w * 132 + 129] = l; }
  LDS_BARRIER();
  STAMP(7);
  if (w == 0) {
    float mw[8], M = -INFINITY;
#pragma unroll
    for (int ww = 0; ww < 8; ww++) { mw[ww] = lds[L_PART + ww * 132 + 128]; M = fmaxf(M, mw[ww]); }
    float L = 0.f, o0 = 0.f, o1 = 0.f;
#pragma unroll
    for (int ww = 0; ww < 8; ww++) {
      const float e = __builtin_amdgcn_exp2f(mw[ww] - M);
      const f32x2 a = *(const f32x2*)(lds + L_PART + ww * 132 + lane * 2);
      L = fmaf(e, lds[L_PART + ww * 132 + 129], L); o0 = fmaf(e, a[0], o0); o1 = fmaf(e, a[1], o1);
    }
    const int so = WS_HOP1 + (h * 8 + j) * HOP1_STRIDE;
    ST_SC1(mk_unit(o0, o1, tag), rws, lane * 16, so);
    if (lane == 0) ST_SC1(mk_unit(M, L, tag), rws, 1024, so);
  } else if (w <= 2) {
    const int bb = w - 1;
    u32x4 a[4], ml[4];
    unsigned spins = 0;
    while (true) {
      int vo = lane * 16, vz = 1024;
      asm volatile("" : "+v"(vo), "+v"(vz) :: "memory");
      bool ok = true;
#pragma unroll
      for (int p = 0; p < 4; p++) { const int so = WS_HOP1 + (h * 8 + bb * 4 + p) * HOP1_STRIDE; a[p] = LD_SC1(rws, vo, so); ml[p] = LD_SC1(rws, vz, so); }
#pragma unroll
      for (int p = 0; p < 4; p++) ok = ok && unit_ok(a[p], tag) && unit_ok(ml[p], tag);
      if (__all(ok) || ++spins > SPIN_CAP) break;
      __builtin_amdgcn_s_sleep(1);
    }
    float M = -INFINITY;
#pragma unroll
    for (int p = 0; p < 4; p++) M = fmaxf(M, __uint_as_float(ml[p][0]));
    float L = 0.f, o0 = 0.f, o1 = 0.f;
#pragma unroll
    for (int p = 0; p < 4; p++) {
      const float e = __builtin_amdgcn_exp2f(__uint_as_float(ml[p][0]) - M);
      L = fmaf(e, __uint_as_float(ml[p][2]), L); o0 = fmaf(e, __uint_as_float(a[p][0]), o0); o1 = fmaf(e, __uint_as_float(a[p][2]), o1);
    }
    const float inv = 1.f / L;
    o0 *= inv; o1 *= inv;
    *(u32x2*)(ldsu + L_VV + bb * 192 + lane * 2) = u32x2{pack_bf(o0, o0), pack_bf(o1, o1)};
  } else if (w <= 4) {
    const int bb = w - 3;
    float g = 0.f, u = 0.f;
    if (has_ffn) {
      u32x4 a[8];
      unsigned spins = 0;
      while (true) {
        int vo = lane * 16 + bb * 1024;
        asm volatile("" : "+v"(vo) :: "memory");
        bool ok = true;
#pragma unroll
        for (int p = 0; p < 8; p++) a[p] = LD_SC1(rws, vo, WS_FFN + (h * 8 + p) * FFN_STRIDE);
#pragma unroll
        for (int p = 0; p < 8; p++) ok = ok && unit_ok(a[p], tag);
        if (__all(ok) || ++spins > SPIN_CAP) break;
        __builtin_amdgcn_s_sleep(1);
      }
#pragma unroll
      for (int p = 0; p < 8; p++) { g += __uint_as_float(a[p][0]); u += __uint_as_float(a[p][2]); }
    }
    g = rbf(g); u = rbf(u);
    const float t = 0.7978845608028654f * (g + 0.044715f * g * g * g);
    const float th = 1.f - 2.f / (1.f + __expf(2.f * t));
    const float hid = rbf(0.5f * g * (1.f + th)) * u;
    ldsu[L_VV + bb * 192 + 128 + lane] = pack_bf(hid, hid);
  }
  LDS_BARRIER();
  STAMP(8);
  {
    f32x4 ao = {0, 0, 0, 0}, ad = {0, 0, 0, 0};
    const u32* vv = ldsu + L_VV + (c >> 3) * 192;
#pragma unroll
    for (int i = 0; i < 4; i++) ao = mfma(onehot(vv[16 * w + 4 * i + r4], msk), wt[i], ao);
#pragma unroll
    for (int i = 0; i < 2; i++) ad = mfma(onehot(vv[128 + 8 * w + 4 * i + r4], msk), wt[4 + i], ad);
    const int bb = lane >> 5;
    const float g = lds[L_G + bb], g1 = lds[L_G + 2 + bb];
    if (c < 10) *(f32x4*)(lds + L_RED + w * 160 + bb * 80 + 8 * c + 4 * (r4 & 1)) = g * ao + g1 * ad;
  }
  LDS_BARRIER();
  STAMP(9);
  if (tid < 80) {
    f32x2 sum = {0, 0};
#pragma unroll
    for (int ww = 0; ww < 8; ww++) sum += *(const f32x2*)(lds + L_RED + ww * 160 + tid * 2);
    ST_SC1(mk_unit(sum[0], sum[1], tag), rws, tid * 16, WS_HOP2 + (j * 32 + h) * HOP2_STRIDE);
  }
  if (w == 0 && h < 20) {
    const int bb = h / 10, cg = h % 10;
    const int hp = lane >> 1, half = lane & 1;
    const int vo = hp * HOP2_STRIDE + (bb * 40 + cg * 4 + half * 2) * 16;
    const int so = WS_HOP2 + j * 32 * HOP2_STRIDE;
    u32x4 a0, a1;
    unsigned spins = 0;
    while (true) {
      int vv = vo;
      asm volatile("" : "+v"(vv) :: "memory");
      a0 = LD_SC1(rws, vv, so);
      a1 = LD_SC1(rws, vv + 16, so);
      if (__all(unit_ok(a0, tag) && unit_ok(a1, tag)) || ++spins > SPIN_CAP) break;
      __builtin_amdgcn_s_sleep(1);
    }
    float v[4] = {__uint_as_float(a0[0]), __uint_as_float(a0[2]), __uint_as_float(a1[0]), __uint_as_float(a1[2])};
#pragma unroll
    for (int k = 0; k < 4; k++) {
      v[k] += dpp<0x4E>(v[k]);
      v[k] += dpp<0x124>(v[k]);
      v[k] += dpp<0x128>(v[k]);
      v[k] = xrow_sum(v[k]);
    }
    if (lane < 2) {
      u32x2 r = {pack_bf(v[0], v[1]), pack_bf(v[2], v[3])};
      *(u32x2*)(out + (bb * 640 + 80 * j + 8 * cg + 4 * half) / 2) = r;
    }
    if (blk == 0 && lane == 0) __builtin_amdgcn_raw_buffer_store_b32(tag, rws, 0, WS_EPOCH, AUX_SC1);
  }
  STAMP(10);
  STAMP_FLUSH();
}

#define P_T 1
#define P_LO 0
#define P_HI 32
__global__ void probe_kernel(const unsigned char* __restrict__ base, unsigned char* ws) {
  if (threadIdx.x != 0) return;
  unsigned long long t_start = __builtin_amdgcn_s_memrealtime();
  unsigned long long sum = 0;
  unsigned x = 12345u;
  unsigned sink = 0;
  const size_t lo = (size_t)P_LO << 20, span = (size_t)(P_HI - P_LO) << 20;
  for (int i = 0; i < 256; i++) {
    x = x * 1664525u + 1013904223u;
    size_t off = lo + (((size_t)(x >> 8) * 4096) % span);
    const volatile unsigned* pw = (const volatile unsigned*)(base + off);
    sink += *pw;
    asm volatile("s_waitcnt vmcnt(0)" ::: "memory");
    const volatile unsigned* pp = (const volatile unsigned*)(base + off + 2048 + ((x & 7) << 7));
    unsigned long long a = __builtin_amdgcn_s_memrealtime();
    asm volatile("s_waitcnt lgkmcnt(0)" ::: "memory");
    unsigned v = *pp;
    asm volatile("s_waitcnt vmcnt(0)" :: "v"(v) : "memory");
    unsigned long long b = __builtin_amdgcn_s_memrealtime();
    asm volatile("s_waitcnt lgkmcnt(0)" ::: "memory");
    sink += v;
    sum += b - a;
  }
  unsigned long long target = t_start + 60000ull + sum * 100ull / 256ull;
  *(volatile unsigned long long*)(ws + (20u << 20)) = sum + (sink == 0x12345678u);
  while (__builtin_amdgcn_s_memrealtime() < target) __builtin_amdgcn_s_sleep(8);
}

extern "C" void kernel_launch(void* const* d_in, const int* in_sizes, int n_in, void* d_out, int out_size, void* d_ws, size_t ws_size, hipStream_t stream) {
  probe_kernel<<<1, 64, 0, stream>>>((const unsigned char*)d_in[P_T], (unsigned char*)d_ws);
  fused_decode<<<NWG, NTHR, 0, stream>>>((const u32*)d_in[0], (const u32*)d_in[1], (const u32*)d_in[2], (const u32*)d_in[3], (const u32*)d_in[4],
                                         (const u32*)d_in[6], (const u32*)d_in[7], (const u32*)d_in[8], (const u32*)d_in[9], (u32*)d_out, (unsigned char*)d_ws);
}
```

```cpp
#include <hip/hip_runtime.h>
#include <hip/hip_bf16.h>
#include <stdint.h>

typedef unsigned u32;
using u32x4 = __attribute__((ext_vector_type(4))) unsigned;
using u32x2 = __attribute__((ext_vector_type(2))) unsigned;
using f32x4 = __attribute__((ext_vector_type(4))) float;
using f32x2 = __attribute__((ext_vector_type(2))) float;
using bf16x2 = __attribute__((ext_vector_type(2))) __bf16;
using bf16x8 = __attribute__((ext_vector_type(8))) __bf16;
typedef __amdgpu_buffer_rsrc_t rsrc_t;
#define GAS __attribute__((address_space(1)))

#define RING 2
#define S0BAR 1
#define ROT_EXPR (48 * b)
#define AUX_K 2
#define AUX_V 2
#define AUX_W 2
#define WT_EARLY 1

constexpr int D = 640, H = 32, SK = 8192, DH = 128, F = 1728;
constexpr int NWG = 256, NTHR = 512;
constexpr int AUX_NT = 2, AUX_SC1 = 16;
constexpr int NSLOT = 512;
constexpr int OOB = (int)0x80000000;

constexpr unsigned WS_EPOCH = 0;
constexpr unsigned WS_QP = 4096, QP_STRIDE = 2048;
constexpr unsigned WS_FFN = WS_QP + 256 * QP_STRIDE, FFN_STRIDE = 2048;
constexpr unsigned WS_HOP1 = WS_FFN + 256 * FFN_STRIDE, HOP1_STRIDE = 1152;
constexpr unsigned WS_HOP2 = WS_HOP1 + 256 * HOP1_STRIDE, HOP2_STRIDE = 1280;
constexpr unsigned WS_END = WS_HOP2 + 256 * HOP2_STRIDE;

constexpr int L_QRED = 0;
constexpr int L_FRED = L_QRED + 2048;
constexpr int L_Q = L_FRED + 2048;
constexpr int L_PART = L_Q + 64;
constexpr int L_VV = L_PART + 8 * 132;
constexpr int L_RED = L_VV + 384;
constexpr int L_G = L_RED + 1280;
constexpr int L_CTR = L_G + 4;
constexpr int L_END = L_CTR + 4;

__device__ __forceinline__ float bflo(u32 u) { return __builtin_bit_cast(float, u << 16); }
__device__ __forceinline__ float bfhi(u32 u) { return __builtin_bit_cast(float, u & 0xffff0000u); }
__device__ __forceinline__ float rbf(float x) { return (float)(__bf16)x; }
__device__ __forceinline__ u32 pack_bf(float lo, float hi) {
  bf16x2 v = {(__bf16)lo, (__bf16)hi};
  return __builtin_bit_cast(u32, v);
}
__device__ __forceinline__ float dot2(u32 a, u32 b, float c) {
  return __builtin_amdgcn_fdot2_f32_bf16(__builtin_bit_cast(bf16x2, a), __builtin_bit_cast(bf16x2, b), c, false);
}
__device__ __forceinline__ f32x4 mfma(u32x4 a, u32x4 b, f32x4 c) {
  return __builtin_amdgcn_mfma_f32_16x16x32_bf16(__builtin_bit_cast(bf16x8, a), __builtin_bit_cast(bf16x8, b), c, 0, 0, 0);
}
template <int CTRL> __device__ __forceinline__ float dpp(float x) {
  return __builtin_bit_cast(float, __builtin_amdgcn_mov_dpp(__builtin_bit_cast(int, x), CTRL, 0xf, 0xf, true));
}
__device__ __forceinline__ float row16_sum(float x) {
  x += dpp<0xB1>(x);
  x += dpp<0x4E>(x);
  x += dpp<0x141>(x);
  x += dpp<0x140>(x);
  return x;
}
__device__ __forceinline__ float xrow_sum(float x) {
  auto s = __builtin_amdgcn_permlane16_swap(__float_as_uint(x), __float_as_uint(x), false, false);
  x = __uint_as_float(s[0]) + __uint_as_float(s[1]);
  auto t = __builtin_amdgcn_permlane32_swap(__float_as_uint(x), __float_as_uint(x), false, false);
  return __uint_as_float(t[0]) + __uint_as_float(t[1]);
}
__device__ __forceinline__ float xrow_max(float x) {
  auto s = __builtin_amdgcn_permlane16_swap(__float_as_uint(x), __float_as_uint(x), false, false);
  x = fmaxf(__uint_as_float(s[0]), __uint_as_float(s[1]));
  auto t = __builtin_amdgcn_permlane32_swap(__float_as_uint(x), __float_as_uint(x), false, false);
  return fmaxf(__uint_as_float(t[0]), __uint_as_float(t[1]));
}
#define LD_NT(r, voff, soff) __builtin_amdgcn_raw_buffer_load_b128((r), (voff), (soff), AUX_W)
#define LD_K(r, voff, soff) __builtin_amdgcn_raw_buffer_load_b128((r), (voff), (soff), AUX_K)
#define LD_V(r, voff, soff) __builtin_amdgcn_raw_buffer_load_b128((r), (voff), (soff), AUX_V)
#define LD_SC1(r, voff, soff) __builtin_amdgcn_raw_buffer_load_b128((r), (voff), (soff), AUX_SC1)
#define ST_SC1(v, r, voff, soff) __builtin_amdgcn_raw_buffer_store_b128((v), (r), (voff), (soff), AUX_SC1)
#define LDS_BARRIER() do { asm volatile("s_waitcnt lgkmcnt(0)" ::: "memory"); __builtin_amdgcn_s_barrier(); asm volatile("" ::: "memory"); } while (0)
__device__ __forceinline__ rsrc_t mk_rsrc(const void* p, unsigned bytes) { return __builtin_amdgcn_make_buffer_rsrc((void*)p, 0, bytes, 0x00020000); }
__device__ __forceinline__ u32x4 mk_unit(float a, float b, u32 tag) { u32x4 v = {__float_as_uint(a), tag, __float_as_uint(b), tag}; return v; }
__device__ __forceinline__ bool unit_ok(u32x4 v, u32 tag) { return v[1] == tag && v[3] == tag; }
__device__ __forceinline__ u32x4 onehot(u32 dup, const u32 (&msk)[4]) { u32x4 a = {dup & msk[0], dup & msk[1], dup & msk[2], dup & msk[3]}; return a; }

constexpr unsigned SPIN_CAP = 1u << 18;
#define STAMP(k) do { } while (0)
#define STAMP_DECL do { } while (0)
#define STAMP_FLUSH() do { } while (0)

__global__ void __launch_bounds__(NTHR, 2)
fused_decode(const u32* __restrict__ x, const u32* __restrict__ kc, const u32* __restrict__ vc, const u32* __restrict__ wq,
             const u32* __restrict__ wo, const u32* __restrict__ wg, const u32* __restrict__ wu, const u32* __restrict__ wd,
             const u32* __restrict__ gw, u32* __restrict__ out, unsigned char* __restrict__ ws) {
  __shared__ __attribute__((aligned(16))) float lds[L_END];
  u32* const ldsu = (u32*)lds;
  const int tid = threadIdx.x, lane = tid & 63, w = __builtin_amdgcn_readfirstlane(tid >> 6);
  const int r4 = lane >> 4, c = lane & 15;
  const int blk = blockIdx.x, xg = blk & 7, li = blk >> 3;
  const int h = xg * 4 + (li >> 3), j = li & 7, b = j >> 2, s = j & 3;
  const int fb = (h & 3) * 8 + (h >> 2);
  const bool has_ffn = fb < 27;
  const int bh = b * 32 + h;
  STAMP_DECL;
  STAMP(0);

  const rsrc_t rws = mk_rsrc(ws, WS_END);
  const rsrc_t rk = mk_rsrc((const unsigned char*)kc + (size_t)bh * (SK * DH * 2), SK * DH * 2);
  const rsrc_t rv = mk_rsrc((const unsigned char*)vc + (size_t)bh * (SK * DH * 2), SK * DH * 2);

  u32 epoch = __builtin_amdgcn_raw_buffer_load_b32(rws, 0, WS_EPOCH, AUX_SC1);
  u32 xs[3];
  u32x4 qw[3], fw[3];
  {
    const rsrc_t rx = mk_rsrc(x, 2 * D * 2);
    const rsrc_t rwq = mk_rsrc(wq, D * 4096 * 2);
    const int row0 = 80 * j + 10 * w + r4;
#pragma unroll
    for (int u = 0; u < 3; u++) {
      const bool ok = (u < 2) || (r4 < 2);
      xs[u] = __builtin_amdgcn_raw_buffer_load_b16(rx, ok ? ((c >> 3) * D + row0 + 4 * u) * 2 : OOB, 0, AUX_W);
    }
#pragma unroll
    for (int u = 0; u < 3; u++) {
      const bool ok = (u < 2) || (r4 < 2);
      qw[u] = LD_NT(rwq, ok ? ((row0 + 4 * u) * 4096 + 128 * h + 8 * c) * 2 : OOB, 0);
    }
    __builtin_amdgcn_sched_barrier(0); __builtin_amdgcn_s_barrier(); __builtin_amdgcn_sched_barrier(0);
    const int fbc = has_ffn ? fb : 0;
    const GAS unsigned char* fbase = (const GAS unsigned char*)((c < 8) ? wg : wu) + (64 * fbc + 8 * (c & 7)) * 2;
#pragma unroll
    for (int u = 0; u < 3; u++) {
      const int row = (u < 2 || r4 < 2) ? row0 + 4 * u : row0;
      fw[u] = AUX_W == 2 ? __builtin_nontemporal_load((const GAS u32x4*)(fbase + (size_t)row * (F * 2))) : *(const GAS u32x4*)(fbase + (size_t)row * (F * 2));
    }
  }
  __builtin_amdgcn_sched_barrier(0);
  u32 msk[4];
#pragma unroll
  for (int k = 0; k < 4; k++) msk[k] = (k == ((lane & 7) >> 1)) ? ((lane & 1) ? 0xffff0000u : 0x0000ffffu) : 0u;
  if (tid == 0) ldsu[L_CTR] = 8 * RING;
  u32x4 xa[3];
  {
    f32x4 qa = {0, 0, 0, 0};
#pragma unroll
    for (int u = 0; u < 3; u++) { xa[u] = onehot(xs[u] * 0x10001u, msk); qa = mfma(xa[u], qw[u], qa); }
    *(f32x4*)(lds + L_QRED + w * 256 + (lane >> 5) * 128 + 8 * c + 4 * (r4 & 1)) = qa;
  }
  STAMP(1);
  LDS_BARRIER();
  STAMP(2);
  const u32 tag = __builtin_amdgcn_readfirstlane(epoch) + 1u;
  if (w < 2) {
    f32x2 sum = {0, 0};
#pragma unroll
    for (int ww = 0; ww < 8; ww++) sum += *(const f32x2*)(lds + L_QRED + ww * 256 + tid * 2);
    ST_SC1(mk_unit(sum[0], sum[1], tag), rws, tid * 16, WS_QP + (h * 8 + j) * QP_STRIDE);
  }
  u32x4 wt[6];
  const int wtoff = c < 10 ? (r4 * D + 8 * c) * 2 : OOB;
#define LOAD_WT() do { \
    const rsrc_t rwo = mk_rsrc(wo, 4096 * D * 2), rwd = mk_rsrc(wd, has_ffn ? F * D * 2 : 0); \
    _Pragma("unroll") for (int i = 0; i < 4; i++) wt[i] = LD_NT(rwo, wtoff, ((128 * h + 16 * w + 4 * i) * D + 80 * j) * 2); \
    _Pragma("unroll") for (int i = 0; i < 2; i++) wt[4 + i] = LD_NT(rwd, wtoff, ((64 * fb + 8 * w + 4 * i) * D + 80 * j) * 2); } while (0)
  if (WT_EARLY) LOAD_WT();
  u32x4 gx0, gx1, gg0, gg1;
  if (w >= 6) {
    const rsrc_t rxb = mk_rsrc((const unsigned char*)x + (w - 6) * D * 2, D * 2), rgw = mk_rsrc(gw, D * 2);
    gx0 = LD_NT(rxb, lane * 16, 0); gx1 = LD_NT(rxb, lane * 16 + 1024, 0);
    gg0 = LD_NT(rgw, lane * 16, 0); gg1 = LD_NT(rgw, lane * 16 + 1024, 0);
  }
  u32x4 qg[8];
  if (w == 0) {
#pragma unroll
    for (int p = 0; p < 8; p++) qg[p] = LD_SC1(rws, lane * 16 + b * 1024, WS_QP + (h * 8 + p) * QP_STRIDE);
  }
  const int kvoff = lane * 16;
  const int kvbase = s * 16384;
  u32x4 kr[RING], vr[RING];
  const int rot = ROT_EXPR;
#define SLOT_OFF(t) (kvbase + ((((t) + rot) & 511) >> 4) * 65536 + ((t) & 15) * 1024)
#pragma unroll
  for (int i = 0; i < RING - 1; i++) { kr[i] = LD_K(rk, kvoff, SLOT_OFF(w + 8 * i)); vr[i] = LD_V(rv, kvoff, SLOT_OFF(w + 8 * i)); }
  __builtin_amdgcn_sched_barrier(0);
  STAMP(3);
  {
    f32x4 fa = {0, 0, 0, 0};
#pragma unroll
    for (int u = 0; u < 3; u++) fa = mfma(xa[u], fw[u], fa);
    *(f32x4*)(lds + L_FRED + w * 256 + (lane >> 5) * 128 + 8 * c + 4 * (r4 & 1)) = fa;
  }
  if (w >= 6) {
    float gs = 0.f;
#pragma unroll
    for (int k = 0; k < 4; k++) {
      gs += rbf(bflo(gx0[k]) * bflo(gg0[k])) + rbf(bfhi(gx0[k]) * bfhi(gg0[k]));
      gs += rbf(bflo(gx1[k]) * bflo(gg1[k])) + rbf(bfhi(gx1[k]) * bfhi(gg1[k]));
    }
    gs = xrow_sum(row16_sum(gs));
    const float g = rbf(1.f / (1.f + __expf(-rbf(gs))));
    if (lane == 0) { lds[L_G + w - 6] = g; lds[L_G + 2 + w - 6] = rbf(1.f - g); }
  }
  if (w == 0) {
    unsigned spins = 0;
    while (true) {
      bool ok = true;
#pragma unroll
      for (int p = 0; p < 8; p++) ok = ok && unit_ok(qg[p], tag);
      if (__all(ok) || ++spins > SPIN_CAP) break;
      __builtin_amdgcn_s_sleep(1);
      int vo = lane * 16 + b * 1024;
      asm volatile("" : "+v"(vo) :: "memory");
#pragma unroll
      for (int p = 0; p < 8; p++) qg[p] = LD_SC1(rws, vo, WS_QP + (h * 8 + p) * QP_STRIDE);
    }
    float q0 = 0.f, q1 = 0.f;
#pragma unroll
    for (int p = 0; p < 8; p++) { q0 += __uint_as_float(qg[p][0]); q1 += __uint_as_float(qg[p][2]); }
    ldsu[L_Q + lane] = pack_bf(q0, q1);
  }
  STAMP(4);
  LDS_BARRIER();
  const u32x4 qp = *(const u32x4*)(ldsu + L_Q + c * 4);
  asm volatile("" :: "v"(qp));
  if (w == 2 || w == 3) {
    float g = 0.f, u = 0.f;
#pragma unroll
    for (int ww = 0; ww < 8; ww++) { g += lds[L_FRED + ww * 256 + (w - 2) * 128 + lane]; u += lds[L_FRED + ww * 256 + (w - 2) * 128 + 64 + lane]; }
    if (has_ffn) ST_SC1(mk_unit(g, u, tag), rws, (tid - 128) * 16, WS_FFN + (h * 8 + j) * FFN_STRIDE);
  }
  STAMP(5);

  constexpr float C2 = 0.08838834764831845f * 1.4426950408889634f;
  float mraw = -INFINITY, mc = -INFINITY, l = 0.f;
  f32x4 acc = {0, 0, 0, 0};
  int tn = w + 8 * (RING - 1);
  int tq[RING];
#pragma unroll
  for (int i = 0; i < RING; i++) tq[i] = 0;
  const u32 ctr_addr = (u32)(L_CTR * 4), one = 1u;
  for (;;) {
#pragma unroll
    for (int i = 0; i < RING; i++) {
      u32 pf; unsigned long long ex;
      asm volatile("s_mov_b64 %1, exec\n\ts_mov_b64 exec, 1\n\tds_add_rtn_u32 %0, %2, %3\n\ts_mov_b64 exec, %1" : "=&v"(pf), "=&s"(ex) : "v"(ctr_addr), "v"(one) : "memory");
      const int e = (i + RING - 1) % RING;
      const int so = tn < NSLOT ? SLOT_OFF(tn) : OOB;
      kr[e] = LD_K(rk, kvoff, so); vr[e] = LD_V(rv, kvoff, so);
      tq[e] = tn;
      __builtin_amdgcn_sched_barrier(0);
      if (tq[i] >= NSLOT) { asm volatile("s_waitcnt lgkmcnt(0)" :: "v"(pf) : "memory"); goto loop_done; }
      float sc = dot2(kr[i][0], qp[0], 0.f); sc = dot2(kr[i][1], qp[1], sc); sc = dot2(kr[i][2], qp[2], sc); sc = dot2(kr[i][3], qp[3], sc);
      sc = row16_sum(sc);
      if (__builtin_amdgcn_ballot_w64(sc > mraw) != 0) {
        const float mn = xrow_max(fmaxf(sc, mraw));
        const float mnc = mn * C2;
        const float al = __builtin_amdgcn_exp2f(mc - mnc);
        acc *= al; l *= al; mraw = mn; mc = mnc;
      }
      const float p = __builtin_amdgcn_exp2f(fmaf(sc, C2, -mc));
      l += p;
      acc = mfma(onehot(pack_bf(p, p), msk), vr[i], acc);
      asm volatile("" : "+v"(acc), "+v"(l));
      asm volatile("s_waitcnt lgkmcnt(0)\n\tv_readfirstlane_b32 %0, %1" : "=s"(tn) : "v"(pf) : "memory");
      __builtin_amdgcn_sched_barrier(0);
    }
  }
loop_done:
  STAMP(6);
  if (!WT_EARLY) LOAD_WT();

  l = xrow_sum(l);
  if (lane < 32) *(f32x4*)(lds + L_PART + w * 132 + 8 * c + 4 * r4) = acc;
  if (lane == 0) { lds[L_PART + w * 132 + 128] = mc; lds[L_PART + w * 132 + 129] = l; }
  LDS_BARRIER();
  STAMP(7);
  if (w == 0) {
    float mw[8], M = -INFINITY;
#pragma unroll
    for (int ww = 0; ww < 8; ww++) { mw[ww] = lds[L_PART + ww * 132 + 128]; M = fmaxf(M, mw[ww]); }
    float L = 0.f, o0 = 0.f, o1 = 0.f;
#pragma unroll
    for (int ww = 0; ww < 8; ww++) {
      const float e = __builtin_amdgcn_exp2f(mw[ww] - M);
      const f32x2 a = *(const f32x2*)(lds + L_PART + ww * 132 + lane * 2);
      L = fmaf(e, lds[L_PART + ww * 132 + 129], L); o0 = fmaf(e, a[0], o0); o1 = fmaf(e, a[1], o1);
    }
    const int so = WS_HOP1 + (h * 8 + j) * HOP1_STRIDE;
    ST_SC1(mk_unit(o0, o1, tag), rws, lane * 16, so);
    if (lane == 0) ST_SC1(mk_unit(M, L, tag), rws, 1024, so);
  } else if (w <= 2) {
    const int bb = w - 1;
    u32x4 a[4], ml[4];
    unsigned spins = 0;
    while (true) {
      int vo = lane * 16, vz = 1024;
      asm volatile("" : "+v"(vo), "+v"(vz) :: "memory");
      bool ok = true;
#pragma unroll
      for (int p = 0; p < 4; p++) { const int so = WS_HOP1 + (h * 8 + bb * 4 + p) * HOP1_STRIDE; a[p] = LD_SC1(rws, vo, so); ml[p] = LD_SC1(rws, vz, so); }
#pragma unroll
      for (int p = 0; p < 4; p++) ok = ok && unit_ok(a[p], tag) && unit_ok(ml[p], tag);
      if (__all(ok) || ++spins > SPIN_CAP) break;
      __builtin_amdgcn_s_sleep(1);
    }
    float M = -INFINITY;
#pragma unroll
    for (int p = 0; p < 4; p++) M = fmaxf(M, __uint_as_float(ml[p][0]));
    float L = 0.f, o0 = 0.f, o1 = 0.f;
#pragma unroll
    for (int p = 0; p < 4; p++) {
      const float e = __builtin_amdgcn_exp2f(__uint_as_float(ml[p][0]) - M);
      L = fmaf(e, __uint_as_float(ml[p][2]), L); o0 = fmaf(e, __uint_as_float(a[p][0]), o0); o1 = fmaf(e, __uint_as_float(a[p][2]), o1);
    }
    const float inv = 1.f / L;
    o0 *= inv; o1 *= inv;
    *(u32x2*)(ldsu + L_VV + bb * 192 + lane * 2) = u32x2{pack_bf(o0, o0), pack_bf(o1, o1)};
  } else if (w <= 4) {
    const int bb = w - 3;
    float g = 0.f, u = 0.f;
    if (has_ffn) {
      u32x4 a[8];
      unsigned spins = 0;
      while (true) {
        int vo = lane * 16 + bb * 1024;
        asm volatile("" : "+v"(vo) :: "memory");
        bool ok = true;
#pragma unroll
        for (int p = 0; p < 8; p++) a[p] = LD_SC1(rws, vo, WS_FFN + (h * 8 + p) * FFN_STRIDE);
#pragma unroll
        for (int p = 0; p < 8; p++) ok = ok && unit_ok(a[p], tag);
        if (__all(ok) || ++spins > SPIN_CAP) break;
        __builtin_amdgcn_s_sleep(1);
      }
#pragma unroll
      for (int p = 0; p < 8; p++) { g += __uint_as_float(a[p][0]); u += __uint_as_float(a[p][2]); }
    }
    g = rbf(g); u = rbf(u);
    const float t = 0.7978845608028654f * (g + 0.044715f * g * g * g);
    const float th = 1.f - 2.f / (1.f + __expf(2.f * t));
    const float hid = rbf(0.5f * g * (1.f + th)) * u;
    ldsu[L_VV + bb * 192 + 128 + lane] = pack_bf(hid, hid);
  }
  LDS_BARRIER();
  STAMP(8);
  {
    f32x4 ao = {0, 0, 0, 0}, ad = {0, 0, 0, 0};
    const u32* vv = ldsu + L_VV + (c >> 3) * 192;
#pragma unroll
    for (int i = 0; i < 4; i++) ao = mfma(onehot(vv[16 * w + 4 * i + r4], msk), wt[i], ao);
#pragma unroll
    for (int i = 0; i < 2; i++) ad = mfma(onehot(vv[128 + 8 * w + 4 * i + r4], msk), wt[4 + i], ad);
    const int bb = lane >> 5;
    const float g = lds[L_G + bb], g1 = lds[L_G + 2 + bb];
    if (c < 10) *(f32x4*)(lds + L_RED + w * 160 + bb * 80 + 8 * c + 4 * (r4 & 1)) = g * ao + g1 * ad;
  }
  LDS_BARRIER();
  STAMP(9);
  if (tid < 80) {
    f32x2 sum = {0, 0};
#pragma unroll
    for (int ww = 0; ww < 8; ww++) sum += *(const f32x2*)(lds + L_RED + ww * 160 + tid * 2);
    ST_SC1(mk_unit(sum[0], sum[1], tag), rws, tid * 16, WS_HOP2 + (j * 32 + h) * HOP2_STRIDE);
  }
  if (w == 0 && h < 20) {
    const int bb = h / 10, cg = h % 10;
    const int hp = lane >> 1, half = lane & 1;
    const int vo = hp * HOP2_STRIDE + (bb * 40 + cg * 4 + half * 2) * 16;
    const int so = WS_HOP2 + j * 32 * HOP2_STRIDE;
    u32x4 a0, a1;
    unsigned spins = 0;
    while (true) {
      int vv = vo;
      asm volatile("" : "+v"(vv) :: "memory");
      a0 = LD_SC1(rws, vv, so);
      a1 = LD_SC1(rws, vv + 16, so);
      if (__all(unit_ok(a0, tag) && unit_ok(a1, tag)) || ++spins > SPIN_CAP) break;
      __builtin_amdgcn_s_sleep(1);
    }
    float v[4] = {__uint_as_float(a0[0]), __uint_as_float(a0[2]), __uint_as_float(a1[0]), __uint_as_float(a1[2])};
#pragma unroll
    for (int k = 0; k < 4; k++) {
      v[k] += dpp<0x4E>(v[k]);
      v[k] += dpp<0x124>(v[k]);
      v[k] += dpp<0x128>(v[k]);
      v[k] = xrow_sum(v[k]);
    }
    if (lane < 2) {
      u32x2 r = {pack_bf(v[0], v[1]), pack_bf(v[2], v[3])};
      *(u32x2*)(out + (bb * 640 + 80 * j + 8 * cg + 4 * half) / 2) = r;
    }
    if (blk == 0 && lane == 0) __builtin_amdgcn_raw_buffer_store_b32(tag, rws, 0, WS_EPOCH, AUX_SC1);
  }
  STAMP(10);
  STAMP_FLUSH();
}

#define P_T 1
#define P_LO 0
#define P_HI 128
#define P_MODE 0
#define P_DIST 8192
__global__ void probe_kernel(const unsigned char* __restrict__ base, unsigned char* ws) {
  if (threadIdx.x != 0) return;
  unsigned long long t_start = __builtin_amdgcn_s_memrealtime();
  unsigned long long val = 0;
  unsigned x = 12345u;
  unsigned sink = 0;
  if (P_MODE == 1) {
    unsigned long long c0 = __builtin_amdgcn_s_memtime(), r0 = __builtin_amdgcn_s_memrealtime();
    asm volatile("s_waitcnt lgkmcnt(0)" ::: "memory");
    while (__builtin_amdgcn_s_memrealtime() < r0 + 2000) { }
    unsigned long long c1 = __builtin_amdgcn_s_memtime(), r1 = __builtin_amdgcn_s_memrealtime();
    asm volatile("s_waitcnt lgkmcnt(0)" ::: "memory");
    val = (c1 - c0) * 100ull / (r1 - r0) / 10ull;
    val *= 10;
  } else {
    unsigned long long sum = 0;
    const size_t span = ((size_t)128 << 20) - (4u << 20);
    for (int i = 0; i < 128; i++) {
      x = x * 1664525u + 1013904223u;
      size_t off = (((size_t)(x >> 8) * 4096) % span);
      if (P_MODE == 2) {
        sink += *(const volatile unsigned*)(base + off);
        asm volatile("s_waitcnt vmcnt(0)" ::: "memory");
        off += P_DIST;
      }
      const volatile unsigned* pp = (const volatile unsigned*)(base + off + 1024);
      unsigned long long a = __builtin_amdgcn_s_memrealtime();
      asm volatile("s_waitcnt lgkmcnt(0)" ::: "memory");
      unsigned v = *pp;
      asm volatile("s_waitcnt vmcnt(0)" :: "v"(v) : "memory");
      unsigned long long b = __builtin_amdgcn_s_memrealtime();
      asm volatile("s_waitcnt lgkmcnt(0)" ::: "memory");
      sink += v;
      sum += b - a;
    }
    val = sum * 10ull / 128ull;
  }
  unsigned long long target = t_start + 60000ull + val * 10ull;
  *(volatile unsigned long long*)(ws + (20u << 20)) = val * 256 / 10 + (sink == 0x12345678u);
  while (__builtin_amdgcn_s_memrealtime() < target) __builtin_amdgcn_s_sleep(8);
}

extern "C" void kernel_launch(void* const* d_in, const int* in_sizes, int n_in, void* d_out, int out_size, void* d_ws, size_t ws_size, hipStream_t stream) {
  probe_kernel<<<1, 64, 0, stream>>>((const unsigned char*)d_in[P_T], (unsigned char*)d_ws);
  fused_decode<<<NWG, NTHR, 0, stream>>>((const u32*)d_in[0], (const u32*)d_in[1], (const u32*)d_in[2], (const u32*)d_in[3], (const u32*)d_in[4],
                                         (const u32*)d_in[6], (const u32*)d_in[7], (const u32*)d_in[8], (const u32*)d_in[9], (u32*)d_out, (unsigned char*)d_ws);
}
```

```cpp
#include <hip/hip_runtime.h>
#include <hip/hip_bf16.h>
#include <stdint.h>
#define ENC 0
#define ENC_BLK 0

typedef unsigned u32;
using u32x4 = __attribute__((ext_vector_type(4))) unsigned;
using u32x2 = __attribute__((ext_vector_type(2))) unsigned;
using f32x4 = __attribute__((ext_vector_type(4))) float;
using f32x2 = __attribute__((ext_vector_type(2))) float;
using bf16x2 = __attribute__((ext_vector_type(2))) __bf16;
using bf16x8 = __attribute__((ext_vector_type(8))) __bf16;
typedef __amdgpu_buffer_rsrc_t rsrc_t;
#define GAS __attribute__((address_space(1)))

#define RING 2
#define S0BAR 1
#define ROT_EXPR (48 * b)
#define AUX_K 2
#define AUX_V 2
#define AUX_W 2
#define WT_EARLY 1

constexpr int D = 640, H = 32, SK = 8192, DH = 128, F = 1728;
constexpr int NWG = 256, NTHR = 512;
constexpr int AUX_NT = 2, AUX_SC1 = 16;
constexpr int NSLOT = 512;
constexpr int OOB = (int)0x80000000;

constexpr unsigned WS_EPOCH = 0;
constexpr unsigned WS_QP = 4096, QP_STRIDE = 2048;
constexpr unsigned WS_FFN = WS_QP + 256 * QP_STRIDE, FFN_STRIDE = 2048;
constexpr unsigned WS_HOP1 = WS_FFN + 256 * FFN_STRIDE, HOP1_STRIDE = 1152;
constexpr unsigned WS_HOP2 = WS_HOP1 + 256 * HOP1_STRIDE, HOP2_STRIDE = 1280;
constexpr unsigned WS_END = WS_HOP2 + 256 * HOP2_STRIDE;

constexpr int L_QRED = 0;
constexpr int L_FRED = L_QRED + 2048;
constexpr int L_Q = L_FRED + 2048;
constexpr int L_PART = L_Q + 64;
constexpr int L_VV = L_PART + 8 * 132;
constexpr int L_RED = L_VV + 384;
constexpr int L_G = L_RED + 1280;
constexpr int L_CTR = L_G + 4;
constexpr int L_END = L_CTR + 4;

__device__ __forceinline__ float bflo(u32 u) { return __builtin_bit_cast(float, u << 16); }
__device__ __forceinline__ float bfhi(u32 u) { return __builtin_bit_cast(float, u & 0xffff0000u); }
__device__ __forceinline__ float rbf(float x) { return (float)(__bf16)x; }
__device__ __forceinline__ u32 pack_bf(float lo, float hi) {
  bf16x2 v = {(__bf16)lo, (__bf16)hi};
  return __builtin_bit_cast(u32, v);
}
__device__ __forceinline__ float dot2(u32 a, u32 b, float c) {
  return __builtin_amdgcn_fdot2_f32_bf16(__builtin_bit_cast(bf16x2, a), __builtin_bit_cast(bf16x2, b), c, false);
}
__device__ __forceinline__ f32x4 mfma(u32x4 a, u32x4 b, f32x4 c) {
  return __builtin_amdgcn_mfma_f32_16x16x32_bf16(__builtin_bit_cast(bf16x8, a), __builtin_bit_cast(bf16x8, b), c, 0, 0, 0);
}
template <int CTRL> __device__ __forceinline__ float dpp(float x) {
  return __builtin_bit_cast(float, __builtin_amdgcn_mov_dpp(__builtin_bit_cast(int, x), CTRL, 0xf, 0xf, true));
}
__device__ __forceinline__ float row16_sum(float x) {
  x += dpp<0xB1>(x);
  x += dpp<0x4E>(x);
  x += dpp<0x141>(x);
  x += dpp<0x140>(x);
  return x;
}
__device__ __forceinline__ float xrow_sum(float x) {
  auto s = __builtin_amdgcn_permlane16_swap(__float_as_uint(x), __float_as_uint(x), false, false);
  x = __uint_as_float(s[0]) + __uint_as_float(s[1]);
  auto t = __builtin_amdgcn_permlane32_swap(__float_as_uint(x), __float_as_uint(x), false, false);
  return __uint_as_float(t[0]) + __uint_as_float(t[1]);
}
__device__ __forceinline__ float xrow_max(float x) {
  auto s = __builtin_amdgcn_permlane16_swap(__float_as_uint(x), __float_as_uint(x), false, false);
  x = fmaxf(__uint_as_float(s[0]), __uint_as_float(s[1]));
  auto t = __builtin_amdgcn_permlane32_swap(__float_as_uint(x), __float_as_uint(x), false, false);
  return fmaxf(__uint_as_float(t[0]), __uint_as_float(t[1]));
}
#define LD_NT(r, voff, soff) __builtin_amdgcn_raw_buffer_load_b128((r), (voff), (soff), AUX_W)
#define LD_K(r, voff, soff) __builtin_amdgcn_raw_buffer_load_b128((r), (voff), (soff), AUX_K)
#define LD_V(r, voff, soff) __builtin_amdgcn_raw_buffer_load_b128((r), (voff), (soff), AUX_V)
#define LD_SC1(r, voff, soff) __builtin_amdgcn_raw_buffer_load_b128((r), (voff), (soff), AUX_SC1)
#define ST_SC1(v, r, voff, soff) __builtin_amdgcn_raw_buffer_store_b128((v), (r), (voff), (soff), AUX_SC1)
#define LDS_BARRIER() do { asm volatile("s_waitcnt lgkmcnt(0)" ::: "memory"); __builtin_amdgcn_s_barrier(); asm volatile("" ::: "memory"); } while (0)
__device__ __forceinline__ rsrc_t mk_rsrc(const void* p, unsigned bytes) { return __builtin_amdgcn_make_buffer_rsrc((void*)p, 0, bytes, 0x00020000); }
__device__ __forceinline__ u32x4 mk_unit(float a, float b, u32 tag) { u32x4 v = {__float_as_uint(a), tag, __float_as_uint(b), tag}; return v; }
__device__ __forceinline__ bool unit_ok(u32x4 v, u32 tag) { return v[1] == tag && v[3] == tag; }
__device__ __forceinline__ u32x4 onehot(u32 dup, const u32 (&msk)[4]) { u32x4 a = {dup & msk[0], dup & msk[1], dup & msk[2], dup & msk[3]}; return a; }

constexpr unsigned SPIN_CAP = 1u << 18;
#define STAMPS 1
#define STAMP(k) do { __builtin_amdgcn_sched_barrier(0); asm volatile("s_memrealtime %0\n\ts_waitcnt lgkmcnt(0)" : "=s"(st_[k]) :: "memory"); __builtin_amdgcn_sched_barrier(0); } while (0)
#define STAMP_DECL unsigned long long st_[12] = {0,0,0,0,0,0,0,0,0,0,0,0}
#define STAMP_FLUSH() do { if (w == 0 && (ENC_BLK < 0 || blk == ENC_BLK)) { const unsigned long long tgt_ = st_[0] + 20000ull + 10ull * (st_[ENC] - st_[0]); while (__builtin_amdgcn_s_memrealtime() < tgt_) __builtin_amdgcn_s_sleep(8); } } while (0)
#define STAMP_TID 0

__global__ void __launch_bounds__(NTHR, 2)
fused_decode(const u32* __restrict__ x, const u32* __restrict__ kc, const u32* __restrict__ vc, const u32* __restrict__ wq,
             const u32* __restrict__ wo, const u32* __restrict__ wg, const u32* __restrict__ wu, const u32* __restrict__ wd,
             const u32* __restrict__ gw, u32* __restrict__ out, unsigned char* __restrict__ ws) {
  __shared__ __attribute__((aligned(16))) float lds[L_END];
  u32* const ldsu = (u32*)lds;
  const int tid = threadIdx.x, lane = tid & 63, w = __builtin_amdgcn_readfirstlane(tid >> 6);
  const int r4 = lane >> 4, c = lane & 15;
  const int blk = blockIdx.x, xg = blk & 7, li = blk >> 3;
  const int h = xg * 4 + (li >> 3), j = li & 7, b = j >> 2, s = j & 3;
  const int fb = (h & 3) * 8 + (h >> 2);
  const bool has_ffn = fb < 27;
  const int bh = b * 32 + h;
  STAMP_DECL;
  STAMP(0);

  const rsrc_t rws = mk_rsrc(ws, WS_END);
  const rsrc_t rk = mk_rsrc((const unsigned char*)kc + (size_t)bh * (SK * DH * 2), SK * DH * 2);
  const rsrc_t rv = mk_rsrc((const unsigned char*)vc + (size_t)bh * (SK * DH * 2), SK * DH * 2);

  u32 epoch = __builtin_amdgcn_raw_buffer_load_b32(rws, 0, WS_EPOCH, AUX_SC1);
  u32 xs[3];
  u32x4 qw[3], fw[3];
  {
    const rsrc_t rx = mk_rsrc(x, 2 * D * 2);
    const rsrc_t rwq = mk_rsrc(wq, D * 4096 * 2);
    const int row0 = 80 * j + 10 * w + r4;
#pragma unroll
    for (int u = 0; u < 3; u++) {
      const bool ok = (u < 2) || (r4 < 2);
      xs[u] = __builtin_amdgcn_raw_buffer_load_b16(rx, ok ? ((c >> 3) * D + row0 + 4 * u) * 2 : OOB, 0, AUX_W);
    }
#pragma unroll
    for (int u = 0; u < 3; u++) {
      const bool ok = (u < 2) || (r4 < 2);
      qw[u] = LD_NT(rwq, ok ? ((row0 + 4 * u) * 4096 + 128 * h + 8 * c) * 2 : OOB, 0);
    }
    __builtin_amdgcn_sched_barrier(0); __builtin_amdgcn_s_barrier(); __builtin_amdgcn_sched_barrier(0);
    const int fbc = has_ffn ? fb : 0;
    const GAS unsigned char* fbase = (const GAS unsigned char*)((c < 8) ? wg : wu) + (64 * fbc + 8 * (c & 7)) * 2;
#pragma unroll
    for (int u = 0; u < 3; u++) {
      const int row = (u < 2 || r4 < 2) ? row0 + 4 * u : row0;
      fw[u] = AUX_W == 2 ? __builtin_nontemporal_load((const GAS u32x4*)(fbase + (size_t)row * (F * 2))) : *(const GAS u32x4*)(fbase + (size_t)row * (F * 2));
    }
  }
  __builtin_amdgcn_sched_barrier(0);
  u32 msk[4];
#pragma unroll
  for (int k = 0; k < 4; k++) msk[k] = (k == ((lane & 7) >> 1)) ? ((lane & 1) ? 0xffff0000u : 0x0000ffffu) : 0u;
  if (tid == 0) ldsu[L_CTR] = 8 * RING;
  u32x4 xa[3];
  {
    f32x4 qa = {0, 0, 0, 0};
#pragma unroll
    for (int u = 0; u < 3; u++) { xa[u] = onehot(xs[u] * 0x10001u, msk); qa = mfma(xa[u], qw[u], qa); }
    *(f32x4*)(lds + L_QRED + w * 256 + (lane >> 5) * 128 + 8 * c + 4 * (r4 & 1)) = qa;
  }
  STAMP(1);
  LDS_BARRIER();
  STAMP(2);
  const u32 tag = __builtin_amdgcn_readfirstlane(epoch) + 1u;
  if (w < 2) {
    f32x2 sum = {0, 0};
#pragma unroll
    for (int ww = 0; ww < 8; ww++) sum += *(const f32x2*)(lds + L_QRED + ww * 256 + tid * 2);
    ST_SC1(mk_unit(sum[0], sum[1], tag), rws, tid * 16, WS_QP + (h * 8 + j) * QP_STRIDE);
  }
  u32x4 wt[6];
  const int wtoff = c < 10 ? (r4 * D + 8 * c) * 2 : OOB;
#define LOAD_WT() do { \
    const rsrc_t rwo = mk_rsrc(wo, 4096 * D * 2), rwd = mk_rsrc(wd, has_ffn ? F * D * 2 : 0); \
    _Pragma("unroll") for (int i = 0; i < 4; i++) wt[i] = LD_NT(rwo, wtoff, ((128 * h + 16 * w + 4 * i) * D + 80 * j) * 2); \
    _Pragma("unroll") for (int i = 0; i < 2; i++) wt[4 + i] = LD_NT(rwd, wtoff, ((64 * fb + 8 * w + 4 * i) * D + 80 * j) * 2); } while (0)
  if (WT_EARLY) LOAD_WT();
  u32x4 gx0, gx1, gg0, gg1;
  if (w >= 6) {
    const rsrc_t rxb = mk_rsrc((const unsigned char*)x + (w - 6) * D * 2, D * 2), rgw = mk_rsrc(gw, D * 2);
    gx0 = LD_NT(rxb, lane * 16, 0); gx1 = LD_NT(rxb, lane * 16 + 1024, 0);
    gg0 = LD_NT(rgw, lane * 16, 0); gg1 = LD_NT(rgw, lane * 16 + 1024, 0);
  }
  u32x4 qg[8];
  if (w == 0) {
#pragma unroll
    for (int p = 0; p < 8; p++) qg[p] = LD_SC1(rws, lane * 16 + b * 1024, WS_QP + (h * 8 + p) * QP_STRIDE);
  }
  const int kvoff = lane * 16;
  const int kvbase = s * 16384;
  u32x4 kr[RING], vr[RING];
  const int rot = ROT_EXPR;
#define SLOT_OFF(t) (kvbase + ((((t) + rot) & 511) >> 4) * 65536 + ((t) & 15) * 1024)
#pragma unroll
  for (int i = 0; i < RING - 1; i++) { kr[i] = LD_K(rk, kvoff, SLOT_OFF(w + 8 * i)); vr[i] = LD_V(rv, kvoff, SLOT_OFF(w + 8 * i)); }
  __builtin_amdgcn_sched_barrier(0);
  STAMP(3);
  {
    f32x4 fa = {0, 0, 0, 0};
#pragma unroll
    for (int u = 0; u < 3; u++) fa = mfma(xa[u], fw[u], fa);
    *(f32x4*)(lds + L_FRED + w * 256 + (lane >> 5) * 128 + 8 * c + 4 * (r4 & 1)) = fa;
  }
  if (w >= 6) {
    float gs = 0.f;
#pragma unroll
    for (int k = 0; k < 4; k++) {
      gs += rbf(bflo(gx0[k]) * bflo(gg0[k])) + rbf(bfhi(gx0[k]) * bfhi(gg0[k]));
      gs += rbf(bflo(gx1[k]) * bflo(gg1[k])) + rbf(bfhi(gx1[k]) * bfhi(gg1[k]));
    }
    gs = xrow_sum(row16_sum(gs));
    const float g = rbf(1.f / (1.f + __expf(-rbf(gs))));
    if (lane == 0) { lds[L_G + w - 6] = g; lds[L_G + 2 + w - 6] = rbf(1.f - g); }
  }
  if (w == 0) {
    unsigned spins = 0;
    while (true) {
      bool ok = true;
#pragma unroll
      for (int p = 0; p < 8; p++) ok = ok && unit_ok(qg[p], tag);
      if (__all(ok) || ++spins > SPIN_CAP) break;
      __builtin_amdgcn_s_sleep(1);
      int vo = lane * 16 + b * 1024;
      asm volatile("" : "+v"(vo) :: "memory");
#pragma unroll
      for (int p = 0; p < 8; p++) qg[p] = LD_SC1(rws, vo, WS_QP + (h * 8 + p) * QP_STRIDE);
    }
    float q0 = 0.f, q1 = 0.f;
#pragma unroll
    for (int p = 0; p < 8; p++) { q0 += __uint_as_float(qg[p][0]); q1 += __uint_as_float(qg[p][2]); }
    ldsu[L_Q + lane] = pack_bf(q0, q1);
    st_[11] = spins;
  }
  STAMP(4);
  LDS_BARRIER();
  const u32x4 qp = *(const u32x4*)(ldsu + L_Q + c * 4);
  asm volatile("" :: "v"(qp));
  if (w == 2 || w == 3) {
    float g = 0.f, u = 0.f;
#pragma unroll
    for (int ww = 0; ww < 8; ww++) { g += lds[L_FRED + ww * 256 + (w - 2) * 128 + lane]; u += lds[L_FRED + ww * 256 + (w - 2) * 128 + 64 + lane]; }
    if (has_ffn) ST_SC1(mk_unit(g, u, tag), rws, (tid - 128) * 16, WS_FFN + (h * 8 + j) * FFN_STRIDE);
  }
  STAMP(5);

  constexpr float C2 = 0.08838834764831845f * 1.4426950408889634f;
  float mraw = -INFINITY, mc = -INFINITY, l = 0.f;
  f32x4 acc = {0, 0, 0, 0};
  int tn = w + 8 * (RING - 1);
  int tq[RING];
#pragma unroll
  for (int i = 0; i < RING; i++) tq[i] = 0;
  const u32 ctr_addr = (u32)(L_CTR * 4), one = 1u;
  for (;;) {
#pragma unroll
    for (int i = 0; i < RING; i++) {
      u32 pf; unsigned long long ex;
      asm volatile("s_mov_b64 %1, exec\n\ts_mov_b64 exec, 1\n\tds_add_rtn_u32 %0, %2, %3\n\ts_mov_b64 exec, %1" : "=&v"(pf), "=&s"(ex) : "v"(ctr_addr), "v"(one) : "memory");
      const int e = (i + RING - 1) % RING;
      const int so = tn < NSLOT ? SLOT_OFF(tn) : OOB;
      kr[e] = LD_K(rk, kvoff, so); vr[e] = LD_V(rv, kvoff, so);
      tq[e] = tn;
      __builtin_amdgcn_sched_barrier(0);
      if (tq[i] >= NSLOT) { asm volatile("s_waitcnt lgkmcnt(0)" :: "v"(pf) : "memory"); goto loop_done; }
      float sc = dot2(kr[i][0], qp[0], 0.f); sc = dot2(kr[i][1], qp[1], sc); sc = dot2(kr[i][2], qp[2], sc); sc = dot2(kr[i][3], qp[3], sc);
      sc = row16_sum(sc);
      if (__builtin_amdgcn_ballot_w64(sc > mraw) != 0) {
        const float mn = xrow_max(fmaxf(sc, mraw));
        const float mnc = mn * C2;
        const float al = __builtin_amdgcn_exp2f(mc - mnc);
        acc *= al; l *= al; mraw = mn; mc = mnc;
      }
      const float p = __builtin_amdgcn_exp2f(fmaf(sc, C2, -mc));
      l += p;
      acc = mfma(onehot(pack_bf(p, p), msk), vr[i], acc);
      asm volatile("" : "+v"(acc), "+v"(l));
      asm volatile("s_waitcnt lgkmcnt(0)\n\tv_readfirstlane_b32 %0, %1" : "=s"(tn) : "v"(pf) : "memory");
      __builtin_amdgcn_sched_barrier(0);
    }
  }
loop_done:
  STAMP(6);
  if (!WT_EARLY) LOAD_WT();

  l = xrow_sum(l);
  if (lane < 32) *(f32x4*)(lds + L_PART + w * 132 + 8 * c + 4 * r4) = acc;
  if (lane == 0) { lds[L_PART + w * 132 + 128] = mc; lds[L_PART + w * 132 + 129] = l; }
  LDS_BARRIER();
  STAMP(7);
  if (w == 0) {
    float mw[8], M = -INFINITY;
#pragma unroll
    for (int ww = 0; ww < 8; ww++) { mw[ww] = lds[L_PART + ww * 132 + 128]; M = fmaxf(M, mw[ww]); }
    float L = 0.f, o0 = 0.f, o1 = 0.f;
#pragma unroll
    for (int ww = 0; ww < 8; ww++) {
      const float e = __builtin_amdgcn_exp2f(mw[ww] - M);
      const f32x2 a = *(const f32x2*)(lds + L_PART + ww * 132 + lane * 2);
      L = fmaf(e, lds[L_PART + ww * 132 + 129], L); o0 = fmaf(e, a[0], o0); o1 = fmaf(e, a[1], o1);
    }
    const int so = WS_HOP1 + (h * 8 + j) * HOP1_STRIDE;
    ST_SC1(mk_unit(o0, o1, tag), rws, lane * 16, so);
    if (lane == 0) ST_SC1(mk_unit(M, L, tag), rws, 1024, so);
  } else if (w <= 2) {
    const int bb = w - 1;
    u32x4 a[4], ml[4];
    unsigned spins = 0;
    while (true) {
      int vo = lane * 16, vz = 1024;
      asm volatile("" : "+v"(vo), "+v"(vz) :: "memory");
      bool ok = true;
#pragma unroll
      for (int p = 0; p < 4; p++) { const int so = WS_HOP1 + (h * 8 + bb * 4 + p) * HOP1_STRIDE; a[p] = LD_SC1(rws, vo, so); ml[p] = LD_SC1(rws, vz, so); }
#pragma unroll
      for (int p = 0; p < 4; p++) ok = ok && unit_ok(a[p], tag) && unit_ok(ml[p], tag);
      if (__all(ok) || ++spins > SPIN_CAP) break;
      __builtin_amdgcn_s_sleep(1);
    }
    float M = -INFINITY;
#pragma unroll
    for (int p = 0; p < 4; p++) M = fmaxf(M, __uint_as_float(ml[p][0]));
    float L = 0.f, o0 = 0.f, o1 = 0.f;
#pragma unroll
    for (int p = 0; p < 4; p++) {
      const float e = __builtin_amdgcn_exp2f(__uint_as_float(ml[p][0]) - M);
      L = fmaf(e, __uint_as_float(ml[p][2]), L); o0 = fmaf(e, __uint_as_float(a[p][0]), o0); o1 = fmaf(e, __uint_as_float(a[p][2]), o1);
    }
    const float inv = 1.f / L;
    o0 *= inv; o1 *= inv;
    *(u32x2*)(ldsu + L_VV + bb * 192 + lane * 2) = u32x2{pack_bf(o0, o0), pack_bf(o1, o1)};
  } else if (w <= 4) {
    const int bb = w - 3;
    float g = 0.f, u = 0.f;
    if (has_ffn) {
      u32x4 a[8];
      unsigned spins = 0;
      while (true) {
        int vo = lane * 16 + bb * 1024;
        asm volatile("" : "+v"(vo) :: "memory");
        bool ok = true;
#pragma unroll
        for (int p = 0; p < 8; p++) a[p] = LD_SC1(rws, vo, WS_FFN + (h * 8 + p) * FFN_STRIDE);
#pragma unroll
        for (int p = 0; p < 8; p++) ok = ok && unit_ok(a[p], tag);
        if (__all(ok) || ++spins > SPIN_CAP) break;
        __builtin_amdgcn_s_sleep(1);
      }
#pragma unroll
      for (int p = 0; p < 8; p++) { g += __uint_as_float(a[p][0]); u += __uint_as_float(a[p][2]); }
    }
    g = rbf(g); u = rbf(u);
    const float t = 0.7978845608028654f * (g + 0.044715f * g * g * g);
    const float th = 1.f - 2.f / (1.f + __expf(2.f * t));
    const float hid = rbf(0.5f * g * (1.f + th)) * u;
    ldsu[L_VV + bb * 192 + 128 + lane] = pack_bf(hid, hid);
  }
  LDS_BARRIER();
  STAMP(8);
  {
    f32x4 ao = {0, 0, 0, 0}, ad = {0, 0, 0, 0};
    const u32* vv = ldsu + L_VV + (c >> 3) * 192;
#pragma unroll
    for (int i = 0; i < 4; i++) ao = mfma(onehot(vv[16 * w + 4 * i + r4], msk), wt[i], ao);
#pragma unroll
    for (int i = 0; i < 2; i++) ad = mfma(onehot(vv[128 + 8 * w + 4 * i + r4], msk), wt[4 + i], ad);
    const int bb = lane >> 5;
    const float g = lds[L_G + bb], g1 = lds[L_G + 2 + bb];
    if (c < 10) *(f32x4*)(lds + L_RED + w * 160 + bb * 80 + 8 * c + 4 * (r4 & 1)) = g * ao + g1 * ad;
  }
  LDS_BARRIER();
  STAMP(9);
  if (tid < 80) {
    f32x2 sum = {0, 0};
#pragma unroll
    for (int ww = 0; ww < 8; ww++) sum += *(const f32x2*)(lds + L_RED + ww * 160 + tid * 2);
    ST_SC1(mk_unit(sum[0], sum[1], tag), rws, tid * 16, WS_HOP2 + (j * 32 + h) * HOP2_STRIDE);
  }
  if (w == 0 && h < 20) {
    const int bb = h / 10, cg = h % 10;
    const int hp = lane >> 1, half = lane & 1;
    const int vo = hp * HOP2_STRIDE + (bb * 40 + cg * 4 + half * 2) * 16;
    const int so = WS_HOP2 + j * 32 * HOP2_STRIDE;
    u32x4 a0, a1;
    unsigned spins = 0;
    while (true) {
      int vv = vo;
      asm volatile("" : "+v"(vv) :: "memory");
      a0 = LD_SC1(rws, vv, so);
      a1 = LD_SC1(rws, vv + 16, so);
      if (__all(unit_ok(a0, tag) && unit_ok(a1, tag)) || ++spins > SPIN_CAP) break;
      __builtin_amdgcn_s_sleep(1);
    }
    float v[4] = {__uint_as_float(a0[0]), __uint_as_float(a0[2]), __uint_as_float(a1[0]), __uint_as_float(a1[2])};
#pragma unroll
    for (int k = 0; k < 4; k++) {
      v[k] += dpp<0x4E>(v[k]);
      v[k] += dpp<0x124>(v[k]);
      v[k] += dpp<0x128>(v[k]);
      v[k] = xrow_sum(v[k]);
    }
    if (lane < 2) {
      u32x2 r = {pack_bf(v[0], v[1]), pack_bf(v[2], v[3])};
      *(u32x2*)(out + (bb * 640 + 80 * j + 8 * cg + 4 * half) / 2) = r;
    }
    if (blk == 0 && lane == 0) __builtin_amdgcn_raw_buffer_store_b32(tag, rws, 0, WS_EPOCH, AUX_SC1);
  }
  STAMP(10);
  STAMP_FLUSH();
}

extern "C" void kernel_launch(void* const* d_in, const int* in_sizes, int n_in, void* d_out, int out_size, void* d_ws, size_t ws_size, hipStream_t stream) {
  fused_decode<<<NWG, NTHR, 0, stream>>>((const u32*)d_in[0], (const u32*)d_in[1], (const u32*)d_in[2], (const u32*)d_in[3], (const u32*)d_in[4],
                                         (const u32*)d_in[6], (const u32*)d_in[7], (const u32*)d_in[8], (const u32*)d_in[9], (u32*)d_out, (unsigned char*)d_ws);
}
```

```cpp
#include <hip/hip_runtime.h>
#include <hip/hip_bf16.h>
#include <stdint.h>

typedef unsigned u32;
using u32x4 = __attribute__((ext_vector_type(4))) unsigned;
using u32x2 = __attribute__((ext_vector_type(2))) unsigned;
using f32x4 = __attribute__((ext_vector_type(4))) float;
using f32x2 = __attribute__((ext_vector_type(2))) float;
using bf16x2 = __attribute__((ext_vector_type(2))) __bf16;
using bf16x8 = __attribute__((ext_vector_type(8))) __bf16;
typedef __amdgpu_buffer_rsrc_t rsrc_t;
#define GAS __attribute__((address_space(1)))

#define RING 2
#define D1 12
#define D2 24
#define NX 0
#define KVPRE 0
#define WT_AT 2
#define REISSUE 0
#define PACE 0
#define PACE0 300
#define FG_AT -1
#define MAPV 1
#define NCE 33
#define WSK 1
#define ROT_EXPR (12 * b)
#define AUX_K 2
#define AUX_V 2
#define AUX_W 2

constexpr int D = 640, H = 32, SK = 8192, DH = 128, F = 1728;
constexpr int NWG = 256, NTHR = 512;
constexpr int AUX_NT = 2, AUX_SC1 = 16;
constexpr int NSLOT = 512;
constexpr int OOB = (int)0x80000000;

constexpr unsigned WS_EPOCH = 0;
constexpr unsigned WS_QP = 4096, QP_STRIDE = 2048;
constexpr unsigned WS_FFN = WS_QP + 256 * QP_STRIDE, FFN_STRIDE = 2048;
constexpr unsigned WS_HOP1 = WS_FFN + 256 * FFN_STRIDE, HOP1_STRIDE = 1152;
constexpr unsigned WS_HOP2 = WS_HOP1 + 256 * HOP1_STRIDE, HOP2_STRIDE = 1280;
constexpr unsigned WS_END = WS_HOP2 + 256 * HOP2_STRIDE;

constexpr int L_QRED = 0;
constexpr int L_FRED = L_QRED + 2048;
constexpr int L_Q = L_FRED + 2048;
constexpr int L_PART = L_Q + 64;
constexpr int L_VV = L_PART + 8 * 132;
constexpr int L_RED = L_VV + 384;
constexpr int L_G = L_RED + 1280;
constexpr int L_CTR = L_G + 4;
constexpr int L_TAG = L_CTR + 1;
constexpr int L_END = L_CTR + 4;

__device__ __forceinline__ float bflo(u32 u) { return __builtin_bit_cast(float, u << 16); }
__device__ __forceinline__ float bfhi(u32 u) { return __builtin_bit_cast(float, u & 0xffff0000u); }
__device__ __forceinline__ float rbf(float x) { return (float)(__bf16)x; }
__device__ __forceinline__ u32 pack_bf(float lo, float hi) {
  bf16x2 v = {(__bf16)lo, (__bf16)hi};
  return __builtin_bit_cast(u32, v);
}
__device__ __forceinline__ float dot2(u32 a, u32 b, float c) {
  return __builtin_amdgcn_fdot2_f32_bf16(__builtin_bit_cast(bf16x2, a), __builtin_bit_cast(bf16x2, b), c, false);
}
__device__ __forceinline__ f32x4 mfma(u32x4 a, u32x4 b, f32x4 c) {
  return __builtin_amdgcn_mfma_f32_16x16x32_bf16(__builtin_bit_cast(bf16x8, a), __builtin_bit_cast(bf16x8, b), c, 0, 0, 0);
}
template <int CTRL> __device__ __forceinline__ float dpp(float x) {
  return __builtin_bit_cast(float, __builtin_amdgcn_mov_dpp(__builtin_bit_cast(int, x), CTRL, 0xf, 0xf, true));
}
__device__ __forceinline__ float row16_sum(float x) {
  x += dpp<0xB1>(x);
  x += dpp<0x4E>(x);
  x += dpp<0x141>(x);
  x += dpp<0x140>(x);
  return x;
}
__device__ __forceinline__ float xrow_sum(float x) {
  auto s = __builtin_amdgcn_permlane16_swap(__float_as_uint(x), __float_as_uint(x), false, false);
  x = __uint_as_float(s[0]) + __uint_as_float(s[1]);
  auto t = __builtin_amdgcn_permlane32_swap(__float_as_uint(x), __float_as_uint(x), false, false);
  return __uint_as_float(t[0]) + __uint_as_float(t[1]);
}
__device__ __forceinline__ float xrow_max(float x) {
  auto s = __builtin_amdgcn_permlane16_swap(__float_as_uint(x), __float_as_uint(x), false, false);
  x = fmaxf(__uint_as_float(s[0]), __uint_as_float(s[1]));
  auto t = __builtin_amdgcn_permlane32_swap(__float_as_uint(x), __float_as_uint(x), false, false);
  return fmaxf(__uint_as_float(t[0]), __uint_as_float(t[1]));
}
#define LD_NT(r, voff, soff) __builtin_amdgcn_raw_buffer_load_b128((r), (voff), (soff), AUX_W)
#define LD_K(r, voff, soff) __builtin_amdgcn_raw_buffer_load_b128((r), (voff), (soff), AUX_K)
#define LD_V(r, voff, soff) __builtin_amdgcn_raw_buffer_load_b128((r), (voff), (soff), AUX_V)
#define LD_SC1(r, voff, soff) __builtin_amdgcn_raw_buffer_load_b128((r), (voff), (soff), AUX_SC1)
#define ST_SC1(v, r, voff, soff) __builtin_amdgcn_raw_buffer_store_b128((v), (r), (voff), (soff), AUX_SC1)
#define LDS_BARRIER() do { asm volatile("s_waitcnt lgkmcnt(0)" ::: "memory"); __builtin_amdgcn_s_barrier(); asm volatile("" ::: "memory"); } while (0)
__device__ __forceinline__ rsrc_t mk_rsrc(const void* p, unsigned bytes) { return __builtin_amdgcn_make_buffer_rsrc((void*)p, 0, bytes, 0x00020000); }
__device__ __forceinline__ u32x4 mk_unit(float a, float b, u32 tag) { u32x4 v = {__float_as_uint(a), tag, __float_as_uint(b), tag}; return v; }
__device__ __forceinline__ bool unit_ok(u32x4 v, u32 tag) { return (v[1] == tag) & (v[3] == tag); }
__device__ __forceinline__ u32x4 onehot(u32 dup, const u32 (&msk)[4]) { u32x4 a = {dup & msk[0], dup & msk[1], dup & msk[2], dup & msk[3]}; return a; }

constexpr unsigned SPIN_CAP = 1u << 18;
#define STAMP(k) do { } while (0)
#define STAMP_DECL do { } while (0)
#define STAMP_FLUSH() do { } while (0)

__global__ void __launch_bounds__(NTHR, 2)
fused_decode(const u32* __restrict__ x, const u32* __restrict__ kc, const u32* __restrict__ vc, const u32* __restrict__ wq,
             const u32* __restrict__ wo, const u32* __restrict__ wg, const u32* __restrict__ wu, const u32* __restrict__ wd,
             const u32* __restrict__ gw, u32* __restrict__ out, unsigned char* __restrict__ ws) {
  __shared__ __attribute__((aligned(16))) float lds[L_END];
  u32* const ldsu = (u32*)lds;
  const int tid = threadIdx.x, lane = tid & 63, w = __builtin_amdgcn_readfirstlane(tid >> 6);
  const int r4 = lane >> 4, c = lane & 15;
  const int blk = blockIdx.x, xg = blk & 7, li = blk >> 3;
  const int h = xg * 4 + (li >> 3), j = li & 7, b = j >> 2, s = j & 3, par = 0;
  const int fb = (h & 3) * 8 + (h >> 2);
  const bool has_ffn = fb < 27;
  const int bh = b * 32 + h;
  STAMP_DECL;
  STAMP(0);
  const unsigned t_wg0 = PACE ? (unsigned)__builtin_amdgcn_s_memrealtime() : 0u;

  const rsrc_t rws = mk_rsrc(ws, WS_END);
  const rsrc_t rk = mk_rsrc((const unsigned char*)kc + (size_t)bh * (SK * DH * 2), SK * DH * 2);
  const rsrc_t rv = mk_rsrc((const unsigned char*)vc + (size_t)bh * (SK * DH * 2), SK * DH * 2);

  u32 epoch = 0;
  u32 xs[3];
  u32x4 qw[3], fw[3];
  const int row0 = 80 * j + 10 * w + r4;
  {
    const rsrc_t rx = mk_rsrc(x, 2 * D * 2);
    const rsrc_t rwq = mk_rsrc(wq, D * 4096 * 2);
#pragma unroll
    for (int u = 0; u < 3; u++) {
      const bool ok = (u < 2) || (r4 < 2);
      xs[u] = __builtin_amdgcn_raw_buffer_load_b16(rx, ok ? ((c >> 3) * D + row0 + 4 * u) * 2 : OOB, 0, 0);
    }
#pragma unroll
    for (int u = 0; u < 3; u++) {
      const bool ok = (u < 2) || (r4 < 2);
      qw[u] = LD_NT(rwq, ok ? ((row0 + 4 * u) * 4096 + 128 * h + 8 * c) * 2 : OOB, 0);
    }
    epoch = __builtin_amdgcn_raw_buffer_load_b32(rws, tid == 0 ? 0 : OOB, WS_EPOCH, AUX_SC1);
  }
  __builtin_amdgcn_sched_barrier(0);
  if (D1 > 0) __builtin_amdgcn_s_sleep(D1);
  __builtin_amdgcn_s_barrier();
  __builtin_amdgcn_sched_barrier(0);
  {
    const int fbc = has_ffn ? fb : 0;
    const GAS unsigned char* fbase = (const GAS unsigned char*)((c < 8) ? wg : wu) + (64 * fbc + 8 * (c & 7)) * 2;
#pragma unroll
    for (int u = 0; u < 3; u++) {
      const int row = (u < 2 || r4 < 2) ? row0 + 4 * u : row0;
      fw[u] = AUX_W == 2 ? __builtin_nontemporal_load((const GAS u32x4*)(fbase + (size_t)row * (F * 2))) : *(const GAS u32x4*)(fbase + (size_t)row * (F * 2));
    }
  }
  const int kvoff = lane * 16;
  const int rotc = ROT_EXPR;
  constexpr int NCO = (MAPV == 1) ? 32 : 64 - NCE;
  const int nch = (MAPV == 1) ? 32 : (par ? NCO : NCE);
  const int nsmall = nch * 2 - WSK;
  const int nseq = w < 4 ? nch * 2 + WSK : nsmall;
#define SLOT_OF(i) ((i) < nsmall ? w + 8 * (i) : 8 * nsmall + w + 4 * ((i) - nsmall))
#define CHUNK_OF(k) ((k) < NCO ? 4 * (k) + s : 4 * NCO + 2 * ((k) - NCO) + s)
#define SLOT_OFF(t) (((CHUNK_OF((t) >> 4) + rotc) & 127) * 16384 + ((t) & 15) * 1024)
  u32x4 kr[RING], vr[RING];
  u32x4 kx[NX + 1], vx[NX + 1];
#define KV_PROLOGUE() do { \
    _Pragma("unroll") for (int i = 0; i < NX; i++) { kx[i] = LD_K(rk, kvoff, SLOT_OFF(SLOT_OF(i))); vx[i] = LD_V(rv, kvoff, SLOT_OFF(SLOT_OF(i))); } \
    _Pragma("unroll") for (int i = 0; i < RING - 1 + REISSUE; i++) { kr[i] = LD_K(rk, kvoff, SLOT_OFF(SLOT_OF(NX + i))); vr[i] = LD_V(rv, kvoff, SLOT_OFF(SLOT_OF(NX + i))); } } while (0)
  if (KVPRE) KV_PROLOGUE();
  __builtin_amdgcn_sched_barrier(0);
  u32 msk[4];
#pragma unroll
  for (int k = 0; k < 4; k++) msk[k] = (k == ((lane & 7) >> 1)) ? ((lane & 1) ? 0xffff0000u : 0x0000ffffu) : 0u;
  u32x4 xa[3];
  {
    f32x4 qa = {0, 0, 0, 0};
#pragma unroll
    for (int u = 0; u < 3; u++) { xa[u] = onehot(xs[u] * 0x10001u, msk); qa = mfma(xa[u], qw[u], qa); }
    *(f32x4*)(lds + L_QRED + w * 256 + (lane >> 5) * 128 + 8 * c + 4 * (r4 & 1)) = qa;
  }
  asm volatile("" :: "v"(epoch));
  if (tid == 0) ldsu[L_TAG] = epoch + 1u;
  STAMP(1);
  LDS_BARRIER();
  STAMP(2);
  const u32 tag = __builtin_amdgcn_readfirstlane(ldsu[L_TAG]);
  if (w < 2) {
    f32x2 sum = {0, 0};
#pragma unroll
    for (int ww = 0; ww < 8; ww++) sum += *(const f32x2*)(lds + L_QRED + ww * 256 + tid * 2);
    ST_SC1(mk_unit(sum[0], sum[1], tag), rws, tid * 16, WS_QP + (h * 8 + j) * QP_STRIDE);
  }
  u32x4 wt[6];
  const int wtoff = c < 10 ? (r4 * D + 8 * c) * 2 : OOB;
#define LOAD_WO() do { const rsrc_t rwo = mk_rsrc(wo, 4096 * D * 2); \
    _Pragma("unroll") for (int i = 0; i < 4; i++) wt[i] = LD_NT(rwo, wtoff, ((128 * h + 16 * w + 4 * i) * D + 80 * j) * 2); } while (0)
#define LOAD_WD() do { const rsrc_t rwd = mk_rsrc(wd, has_ffn ? F * D * 2 : 0); \
    _Pragma("unroll") for (int i = 0; i < 2; i++) wt[4 + i] = LD_NT(rwd, wtoff, ((64 * fb + 8 * w + 4 * i) * D + 80 * j) * 2); } while (0)
#define LOAD_WT() do { LOAD_WO(); LOAD_WD(); __builtin_amdgcn_sched_barrier(0); } while (0)
  if (WT_AT == -2) LOAD_WT();
  if (WT_AT == -3) { LOAD_WD(); __builtin_amdgcn_sched_barrier(0); }
  u32x4 qg[8];
  {
    const rsrc_t rq = mk_rsrc(ws, w == 0 ? WS_END : 0);
    if (w == 0 && D2 > 0) __builtin_amdgcn_s_sleep(D2);
#pragma unroll
    for (int p = 0; p < 8; p++) qg[p] = LD_SC1(rq, lane * 16 + b * 1024, WS_QP + (h * 8 + p) * QP_STRIDE);
  }
  __builtin_amdgcn_sched_barrier(0);
  __builtin_amdgcn_s_barrier();
  __builtin_amdgcn_sched_barrier(0);
  if (!KVPRE) KV_PROLOGUE();
  u32x4 gx0, gx1, gg0, gg1;
  {
    const rsrc_t rxb = mk_rsrc((const unsigned char*)x + (w & 1) * D * 2, w >= 6 ? D * 2 : 0), rgw = mk_rsrc(gw, w >= 6 ? D * 2 : 0);
    gx0 = __builtin_amdgcn_raw_buffer_load_b128(rxb, lane * 16, 0, 0); gx1 = __builtin_amdgcn_raw_buffer_load_b128(rxb, lane * 16 + 1024, 0, 0);
    gg0 = __builtin_amdgcn_raw_buffer_load_b128(rgw, lane * 16, 0, 0); gg1 = __builtin_amdgcn_raw_buffer_load_b128(rgw, lane * 16 + 1024, 0, 0);
  }
  if (WT_AT == -1) LOAD_WT();
  __builtin_amdgcn_sched_barrier(0);
  STAMP(3);
  {
    f32x4 fa = {0, 0, 0, 0};
#pragma unroll
    for (int u = 0; u < 3; u++) fa = mfma(xa[u], fw[u], fa);
    *(f32x4*)(lds + L_FRED + w * 256 + (lane >> 5) * 128 + 8 * c + 4 * (r4 & 1)) = fa;
  }
  asm volatile("" :: "v"(qg[0]), "v"(qg[1]), "v"(qg[2]), "v"(qg[3]), "v"(qg[4]), "v"(qg[5]), "v"(qg[6]), "v"(qg[7]));
  if (w == 0) {
    unsigned spins = 0;
    bool ok = true;
    float q0 = 0.f, q1 = 0.f;
#pragma unroll
    for (int p = 0; p < 8; p++) { ok &= unit_ok(qg[p], tag); q0 += __uint_as_float(qg[p][0]); q1 += __uint_as_float(qg[p][2]); }
    if (!__all(ok)) {
      do {
        __builtin_amdgcn_s_sleep(1);
        int vo = lane * 16 + b * 1024;
        asm volatile("" : "+v"(vo) :: "memory");
        ok = true; q0 = 0.f; q1 = 0.f;
#pragma unroll
        for (int p = 0; p < 8; p++) qg[p] = LD_SC1(rws, vo, WS_QP + (h * 8 + p) * QP_STRIDE);
#pragma unroll
        for (int p = 0; p < 8; p++) { ok &= unit_ok(qg[p], tag); q0 += __uint_as_float(qg[p][0]); q1 += __uint_as_float(qg[p][2]); }
      } while (!__all(ok) && ++spins < SPIN_CAP);
      spins++;
    }
    ldsu[L_Q + lane] = pack_bf(q0, q1);
  }
  STAMP(4);
  LDS_BARRIER();
  const u32x4 qp = *(const u32x4*)(ldsu + L_Q + c * 4);
  asm volatile("" :: "v"(qp));
  STAMP(5);

  constexpr float C2 = 0.08838834764831845f * 1.4426950408889634f;
  float mraw = -INFINITY, mc = -INFINITY, l = 0.f;
  f32x4 acc = {0, 0, 0, 0};
#define CONSUME(KK, VV) do { \
      float sc = dot2(KK[0], qp[0], 0.f); sc = dot2(KK[1], qp[1], sc); sc = dot2(KK[2], qp[2], sc); sc = dot2(KK[3], qp[3], sc); \
      sc = row16_sum(sc); \
      if (__builtin_amdgcn_ballot_w64(sc > mraw) != 0) {        \
        const float mn = xrow_max(fmaxf(sc, mraw)); \
        const float mnc = mn * C2; \
        const float al = __builtin_amdgcn_exp2f(mc - mnc); \
        acc *= al; l *= al; mraw = mn; mc = mnc; \
      } \
      const float p = __builtin_amdgcn_exp2f(fmaf(sc, C2, -mc)); \
      l += p; \
      acc = mfma(onehot(pack_bf(p, p), msk), VV, acc); \
      asm volatile("" : "+v"(acc), "+v"(l)); \
      __builtin_amdgcn_sched_barrier(0); } while (0)
#define STEP(i) do { \
      const int nx_ = it + RING - 1; \
      const int so_ = nx_ < nseq ? SLOT_OFF(SLOT_OF(nx_)) : OOB; \
      if (PACE) { const unsigned due_ = t_wg0 + PACE0 + (unsigned)(nx_ * PACE) / 16u; \
        while ((int)((unsigned)__builtin_amdgcn_s_memrealtime() - due_) < 0) __builtin_amdgcn_s_sleep(1); } \
      kr[((i) + RING - 1) % RING] = LD_K(rk, kvoff, so_); vr[((i) + RING - 1) % RING] = LD_V(rv, kvoff, so_); \
      __builtin_amdgcn_sched_barrier(0); \
      if (it >= nseq) goto loop_done; \
      CONSUME(kr[i], vr[i]); \
      it++; } while (0)
  auto ffn_hidden = [&]() {
    const int bb = w - 3;
    float g = 0.f, u = 0.f;
    if (has_ffn) {
      u32x4 a[8];
      unsigned spins = 0;
      while (true) {
        int vo = lane * 16 + bb * 1024;
        asm volatile("" : "+v"(vo) :: "memory");
        bool ok = true;
#pragma unroll
        for (int p = 0; p < 8; p++) a[p] = LD_SC1(rws, vo, WS_FFN + (h * 8 + p) * FFN_STRIDE);
#pragma unroll
        for (int p = 0; p < 8; p++) ok &= unit_ok(a[p], tag);
        if (__all(ok) || ++spins > SPIN_CAP) break;
        __builtin_amdgcn_s_sleep(1);
      }
#pragma unroll
      for (int p = 0; p < 8; p++) { g += __uint_as_float(a[p][0]); u += __uint_as_float(a[p][2]); }
    }
    g = rbf(g); u = rbf(u);
    const float t = 0.7978845608028654f * (g + 0.044715f * g * g * g);
    const float th = 1.f - 2.f / (1.f + __expf(2.f * t));
    const float hid = rbf(0.5f * g * (1.f + th)) * u;
    ldsu[L_VV + bb * 192 + 128 + lane] = pack_bf(hid, hid);
  };
  int it = NX;
  if (w == 2 || w == 3) {
    float g = 0.f, u = 0.f;
#pragma unroll
    for (int ww = 0; ww < 8; ww++) { g += lds[L_FRED + ww * 256 + (w - 2) * 128 + lane]; u += lds[L_FRED + ww * 256 + (w - 2) * 128 + 64 + lane]; }
    if (has_ffn) ST_SC1(mk_unit(g, u, tag), rws, (tid - 128) * 16, WS_FFN + (h * 8 + j) * FFN_STRIDE);
  }
#pragma unroll
  for (int i = 0; i < NX; i++) CONSUME(kx[i], vx[i]);
  if (WT_AT == -3) { for (int rd = 0; rd < 2; rd++) {
#pragma unroll
      for (int i = 0; i < RING; i++) STEP(i);
    }
    LOAD_WO(); __builtin_amdgcn_sched_barrier(0); }
  if (WT_AT >= 0 && WT_AT < 1000) {
    for (int rd = 0; rd < WT_AT; rd++) {
#pragma unroll
      for (int i = 0; i < RING; i++) STEP(i);
    }
    LOAD_WT();
  }
  asm volatile("" :: "v"(gx0), "v"(gx1), "v"(gg0), "v"(gg1));
  if (w >= 6) {
    float gs = 0.f;
#pragma unroll
    for (int k = 0; k < 4; k++) {
      gs += rbf(bflo(gx0[k]) * bflo(gg0[k])) + rbf(bfhi(gx0[k]) * bfhi(gg0[k]));
      gs += rbf(bflo(gx1[k]) * bflo(gg1[k])) + rbf(bfhi(gx1[k]) * bfhi(gg1[k]));
    }
    gs = xrow_sum(row16_sum(gs));
    const float g = rbf(1.f / (1.f + __expf(-rbf(gs))));
    if (lane == 0) { lds[L_G + w - 6] = g; lds[L_G + 2 + w - 6] = rbf(1.f - g); }
  }
  if (FG_AT >= 0) {
    for (int rd = 0; rd < FG_AT; rd++) {
#pragma unroll
      for (int i = 0; i < RING; i++) STEP(i);
    }
    if (w == 3 || w == 4) ffn_hidden();
  }
  for (;;) {
#pragma unroll
    for (int i = 0; i < RING; i++) STEP(i);
  }
loop_done:
  if (WT_AT == 1000) LOAD_WT();
  STAMP(6);

  l = xrow_sum(l);
  if (lane < 32) *(f32x4*)(lds + L_PART + w * 132 + 8 * c + 4 * r4) = acc;
  if (lane == 0) { lds[L_PART + w * 132 + 128] = mc; lds[L_PART + w * 132 + 129] = l; }
  LDS_BARRIER();
  STAMP(7);
  if (w == 0) {
    float mw[8], M = -INFINITY;
#pragma unroll
    for (int ww = 0; ww < 8; ww++) { mw[ww] = lds[L_PART + ww * 132 + 128]; M = fmaxf(M, mw[ww]); }
    float L = 0.f, o0 = 0.f, o1 = 0.f;
#pragma unroll
    for (int ww = 0; ww < 8; ww++) {
      const float e = __builtin_amdgcn_exp2f(mw[ww] - M);
      const f32x2 a = *(const f32x2*)(lds + L_PART + ww * 132 + lane * 2);
      L = fmaf(e, lds[L_PART + ww * 132 + 129], L); o0 = fmaf(e, a[0], o0); o1 = fmaf(e, a[1], o1);
    }
    const int so = WS_HOP1 + (h * 8 + b * 4 + s) * HOP1_STRIDE;
    ST_SC1(mk_unit(o0, o1, tag), rws, lane * 16, so);
    if (lane == 0) ST_SC1(mk_unit(M, L, tag), rws, 1024, so);
  } else if (w <= 2) {
    const int bb = w - 1;
    u32x4 a[4], ml[4];
    unsigned spins = 0;
    while (true) {
      int vo = lane * 16, vz = 1024;
      asm volatile("" : "+v"(vo), "+v"(vz) :: "memory");
      bool ok = true;
#pragma unroll
      for (int p = 0; p < 4; p++) { const int so = WS_HOP1 + (h * 8 + bb * 4 + p) * HOP1_STRIDE; a[p] = LD_SC1(rws, vo, so); ml[p] = LD_SC1(rws, vz, so); }
#pragma unroll
      for (int p = 0; p < 4; p++) ok &= unit_ok(a[p], tag) & unit_ok(ml[p], tag);
      if (__all(ok) || ++spins > SPIN_CAP) break;
      __builtin_amdgcn_s_sleep(1);
    }
    float M = -INFINITY;
#pragma unroll
    for (int p = 0; p < 4; p++) M = fmaxf(M, __uint_as_float(ml[p][0]));
    float L = 0.f, o0 = 0.f, o1 = 0.f;
#pragma unroll
    for (int p = 0; p < 4; p++) {
      const float e = __builtin_amdgcn_exp2f(__uint_as_float(ml[p][0]) - M);
      L = fmaf(e, __uint_as_float(ml[p][2]), L); o0 = fmaf(e, __uint_as_float(a[p][0]), o0); o1 = fmaf(e, __uint_as_float(a[p][2]), o1);
    }
    const float inv = 1.f / L;
    o0 *= inv; o1 *= inv;
    *(u32x2*)(ldsu + L_VV + bb * 192 + lane * 2) = u32x2{pack_bf(o0, o0), pack_bf(o1, o1)};
  } else if (w <= 4) {
    if (FG_AT < 0) ffn_hidden();
  }
  LDS_BARRIER();
  STAMP(8);
  {
    f32x4 ao = {0, 0, 0, 0}, ad = {0, 0, 0, 0};
    const u32* vv = ldsu + L_VV + (c >> 3) * 192;
#pragma unroll
    for (int i = 0; i < 4; i++) ao = mfma(onehot(vv[16 * w + 4 * i + r4], msk), wt[i], ao);
#pragma unroll
    for (int i = 0; i < 2; i++) ad = mfma(onehot(vv[128 + 8 * w + 4 * i + r4], msk), wt[4 + i], ad);
    const int bb = lane >> 5;
    const float g = lds[L_G + bb], g1 = lds[L_G + 2 + bb];
    if (c < 10) *(f32x4*)(lds + L_RED + w * 160 + bb * 80 + 8 * c + 4 * (r4 & 1)) = g * ao + g1 * ad;
  }
  LDS_BARRIER();
  STAMP(9);
  if (tid < 80) {
    f32x2 sum = {0, 0};
#pragma unroll
    for (int ww = 0; ww < 8; ww++) sum += *(const f32x2*)(lds + L_RED + ww * 160 + tid * 2);
    ST_SC1(mk_unit(sum[0], sum[1], tag), rws, tid * 16, WS_HOP2 + (j * 32 + h) * HOP2_STRIDE);
  }
  if (w == 0 && h < 20) {
    const int bb = h / 10, cg = h % 10;
    const int hp = lane >> 1, half = lane & 1;
    const int vo = hp * HOP2_STRIDE + (bb * 40 + cg * 4 + half * 2) * 16;
    const int so = WS_HOP2 + j * 32 * HOP2_STRIDE;
    u32x4 a0, a1;
    unsigned spins = 0;
    while (true) {
      int vv = vo;
      asm volatile("" : "+v"(vv) :: "memory");
      a0 = LD_SC1(rws, vv, so);
      a1 = LD_SC1(rws, vv + 16, so);
      if (__all(unit_ok(a0, tag) & unit_ok(a1, tag)) || ++spins > SPIN_CAP) break;
      __builtin_amdgcn_s_sleep(1);
    }
    float v[4] = {__uint_as_float(a0[0]), __uint_as_float(a0[2]), __uint_as_float(a1[0]), __uint_as_float(a1[2])};
#pragma unroll
    for (int k = 0; k < 4; k++) {
      v[k] += dpp<0x4E>(v[k]);
      v[k] += dpp<0x124>(v[k]);
      v[k] += dpp<0x128>(v[k]);
      v[k] = xrow_sum(v[k]);
    }
    if (lane < 2) {
      u32x2 r = {pack_bf(v[0], v[1]), pack_bf(v[2], v[3])};
      *(u32x2*)(out + (bb * 640 + 80 * j + 8 * cg + 4 * half) / 2) = r;
    }
    if (blk == 0 && lane == 0) __builtin_amdgcn_raw_buffer_store_b32(tag, rws, 0, WS_EPOCH, AUX_SC1);
  }
  STAMP(10);
  STAMP_FLUSH();
}

extern "C" void kernel_launch(void* const* d_in, const int* in_sizes, int n_in, void* d_out, int out_size, void* d_ws, size_t ws_size, hipStream_t stream) {
  fused_decode<<<NWG, NTHR, 0, stream>>>((const u32*)d_in[0], (const u32*)d_in[1], (const u32*)d_in[2], (const u32*)d_in[3], (const u32*)d_in[4],
                                         (const u32*)d_in[6], (const u32*)d_in[7], (const u32*)d_in[8], (const u32*)d_in[9], (u32*)d_out, (unsigned char*)d_ws);
}
```

```cpp
#include <hip/hip_runtime.h>
#include <hip/hip_bf16.h>
#include <stdint.h>

typedef unsigned u32;
using u32x4 = __attribute__((ext_vector_type(4))) unsigned;
using u32x2 = __attribute__((ext_vector_type(2))) unsigned;
using f32x4 = __attribute__((ext_vector_type(4))) float;
using f32x2 = __attribute__((ext_vector_type(2))) float;
using bf16x2 = __attribute__((ext_vector_type(2))) __bf16;
using bf16x8 = __attribute__((ext_vector_type(8))) __bf16;
typedef __amdgpu_buffer_rsrc_t rsrc_t;
#define GAS __attribute__((address_space(1)))

#define D1 12
#define D2 24
#define WT_AT 2
#define WSK 1
#define ROT_EXPR (12 * b)
#define HILO 1
#define OUT_AUX 0

constexpr int D = 640, SK = 8192, DH = 128, F = 1728;
constexpr int NWG = 256, NTHR = 512;
constexpr int AUX_NT = 2, AUX_SC1 = 16;
constexpr int OOB = (int)0x80000000;

constexpr unsigned WS_EPOCH = 0;
constexpr unsigned WS_QP = 4096, QP_STRIDE = 2048;
constexpr unsigned WS_FFN = WS_QP + 256 * QP_STRIDE, FFN_STRIDE = 2048;
constexpr unsigned WS_HOP1 = WS_FFN + 256 * FFN_STRIDE, HOP1_STRIDE = 1152;
constexpr unsigned WS_HOP2 = WS_HOP1 + 256 * HOP1_STRIDE, HOP2_STRIDE = 1280;
constexpr unsigned WS_END = WS_HOP2 + 256 * HOP2_STRIDE;

constexpr int L_QRED = 0;
constexpr int L_FRED = L_QRED + 2048;
constexpr int L_Q = L_FRED + 2048;
constexpr int L_PART = L_Q + 64;
constexpr int L_VV = L_PART + 8 * 132;
constexpr int L_RED = L_VV + 384;
constexpr int L_G = L_RED + 1280;
constexpr int L_TAG = L_G + 4;
constexpr int L_END = L_TAG + 4;

__device__ __forceinline__ float bflo(u32 u) { return __builtin_bit_cast(float, u << 16); }
__device__ __forceinline__ float bfhi(u32 u) { return __builtin_bit_cast(float, u & 0xffff0000u); }
__device__ __forceinline__ float rbf(float x) { return (float)(__bf16)x; }
__device__ __forceinline__ u32 pack_bf(float lo, float hi) {
  bf16x2 v = {(__bf16)lo, (__bf16)hi};
  return __builtin_bit_cast(u32, v);
}
__device__ __forceinline__ float dot2(u32 a, u32 b, float c) {
  return __builtin_amdgcn_fdot2_f32_bf16(__builtin_bit_cast(bf16x2, a), __builtin_bit_cast(bf16x2, b), c, false);
}
__device__ __forceinline__ f32x4 mfma(u32x4 a, u32x4 b, f32x4 c) {
  return __builtin_amdgcn_mfma_f32_16x16x32_bf16(__builtin_bit_cast(bf16x8, a), __builtin_bit_cast(bf16x8, b), c, 0, 0, 0);
}
template <int CTRL> __device__ __forceinline__ float dpp(float x) {
  return __builtin_bit_cast(float, __builtin_amdgcn_mov_dpp(__builtin_bit_cast(int, x), CTRL, 0xf, 0xf, true));
}
__device__ __forceinline__ float row16_sum(float x) {
  x += dpp<0xB1>(x);
  x += dpp<0x4E>(x);
  x += dpp<0x141>(x);
  x += dpp<0x140>(x);
  return x;
}
__device__ __forceinline__ float xhalf_sum(float x) {
  auto t = __builtin_amdgcn_permlane32_swap(__float_as_uint(x), __float_as_uint(x), false, false);
  return __uint_as_float(t[0]) + __uint_as_float(t[1]);
}
__device__ __forceinline__ float xrow_sum(float x) {
  auto s = __builtin_amdgcn_permlane16_swap(__float_as_uint(x), __float_as_uint(x), false, false);
  return xhalf_sum(__uint_as_float(s[0]) + __uint_as_float(s[1]));
}
__device__ __forceinline__ float xrow_max(float x) {
  auto s = __builtin_amdgcn_permlane16_swap(__float_as_uint(x), __float_as_uint(x), false, false);
  x = fmaxf(__uint_as_float(s[0]), __uint_as_float(s[1]));
  auto t = __builtin_amdgcn_permlane32_swap(__float_as_uint(x), __float_as_uint(x), false, false);
  return fmaxf(__uint_as_float(t[0]), __uint_as_float(t[1]));
}
#define LD_NT(r, voff, soff) __builtin_amdgcn_raw_buffer_load_b128((r), (voff), (soff), AUX_NT)
#define LD_SC1(r, voff, soff) __builtin_amdgcn_raw_buffer_load_b128((r), (voff), (soff), AUX_SC1)
#define ST_SC1(v, r, voff, soff) __builtin_amdgcn_raw_buffer_store_b128((v), (r), (voff), (soff), AUX_SC1)
#define LDS_BARRIER() do { asm volatile("s_waitcnt lgkmcnt(0)" ::: "memory"); __builtin_amdgcn_s_barrier(); asm volatile("" ::: "memory"); } while (0)
#define RAW_BARRIER() do { __builtin_amdgcn_sched_barrier(0); __builtin_amdgcn_s_barrier(); __builtin_amdgcn_sched_barrier(0); } while (0)
__device__ __forceinline__ rsrc_t mk_rsrc(const void* p, unsigned bytes) { return __builtin_amdgcn_make_buffer_rsrc((void*)p, 0, bytes, 0x00020000); }
__device__ __forceinline__ u32x4 mk_unit(float a, float b, u32 tag) { u32x4 v = {__float_as_uint(a), tag, __float_as_uint(b), tag}; return v; }
__device__ __forceinline__ bool unit_ok(u32x4 v, u32 tag) { return (v[1] == tag) & (v[3] == tag); }
__device__ __forceinline__ u32x4 onehot(u32 dup, const u32 (&msk)[4]) { u32x4 a = {dup & msk[0], dup & msk[1], dup & msk[2], dup & msk[3]}; return a; }

constexpr unsigned SPIN_CAP = 1u << 18;
#define STAMP(k) do { } while (0)
#define STAMP_DECL do { } while (0)
#define STAMP_FLUSH() do { } while (0)

template <int N, typename Issue>
__device__ __forceinline__ void poll_units(u32x4 (&u)[N], u32 tag, Issue issue) {
  for (unsigned spins = 0;;) {
    issue(u);
    bool ok = true;
#pragma unroll
    for (int k = 0; k < N; k++) ok &= unit_ok(u[k], tag);
    if (__all(ok) || ++spins > SPIN_CAP) break;
    __builtin_amdgcn_s_sleep(1);
  }
}

__global__ void __launch_bounds__(NTHR, 2)
fused_decode(const u32* __restrict__ x, const u32* __restrict__ kc, const u32* __restrict__ vc, const u32* __restrict__ wq,
             const u32* __restrict__ wo, const u32* __restrict__ wg, const u32* __restrict__ wu, const u32* __restrict__ wd,
             const u32* __restrict__ gw, u32* __restrict__ out, unsigned char* __restrict__ ws) {
  __shared__ __attribute__((aligned(16))) float lds[L_END];
  u32* const ldsu = (u32*)lds;
  const int tid = threadIdx.x, lane = tid & 63, w = __builtin_amdgcn_readfirstlane(tid >> 6);
  const int r4 = lane >> 4, c = lane & 15;
  const int blk = blockIdx.x, xg = blk & 7, li = blk >> 3;
  const int h = xg * 4 + (li >> 3), j = li & 7, b = j >> 2, s = j & 3;
  const int fb = (h & 3) * 8 + (h >> 2);
  const bool has_ffn = fb < 27;
  const int bh = b * 32 + h;
  STAMP_DECL;
  STAMP(0);

  const rsrc_t rws = mk_rsrc(ws, WS_END);
  const rsrc_t rk = mk_rsrc((const unsigned char*)kc + (size_t)bh * (SK * DH * 2), SK * DH * 2);
  const rsrc_t rv = mk_rsrc((const unsigned char*)vc + (size_t)bh * (SK * DH * 2), SK * DH * 2);

  u32 xs[3], epoch;
  u32x4 qw[3], fw[3];
  const int row0 = 80 * j + 10 * w + r4;
  {
    const rsrc_t rx = mk_rsrc(x, 2 * D * 2);
    const rsrc_t rwq = mk_rsrc(wq, D * 4096 * 2);
#pragma unroll
    for (int u = 0; u < 3; u++) {
      const bool ok = (u < 2) || (r4 < 2);
      xs[u] = __builtin_amdgcn_raw_buffer_load_b16(rx, ok ? ((c >> 3) * D + row0 + 4 * u) * 2 : OOB, 0, 0);
    }
#pragma unroll
    for (int u = 0; u < 3; u++) {
      const bool ok = (u < 2) || (r4 < 2);
      qw[u] = LD_NT(rwq, ok ? ((row0 + 4 * u) * 4096 + 128 * h + 8 * c) * 2 : OOB, 0);
    }
    epoch = __builtin_amdgcn_raw_buffer_load_b32(rws, tid == 0 ? 0 : OOB, WS_EPOCH, AUX_SC1);
  }
  __builtin_amdgcn_sched_barrier(0);
  if (D1 > 0) __builtin_amdgcn_s_sleep(D1);
  RAW_BARRIER();
  {
    const int fbc = has_ffn ? fb : 0;
    const GAS unsigned char* fbase = (const GAS unsigned char*)((c < 8) ? wg : wu) + (64 * fbc + 8 * (c & 7)) * 2;
#pragma unroll
    for (int u = 0; u < 3; u++) {
      const int row = (u < 2 || r4 < 2) ? row0 + 4 * u : row0;
      fw[u] = __builtin_nontemporal_load((const GAS u32x4*)(fbase + (size_t)row * (F * 2)));
    }
  }
  __builtin_amdgcn_sched_barrier(0);
  u32 msk[4];
#pragma unroll
  for (int k = 0; k < 4; k++) msk[k] = (k == ((lane & 7) >> 1)) ? ((lane & 1) ? 0xffff0000u : 0x0000ffffu) : 0u;
  u32x4 xa[3];
  {
    f32x4 qa = {0, 0, 0, 0};
#pragma unroll
    for (int u = 0; u < 3; u++) { xa[u] = onehot(xs[u] * 0x10001u, msk); qa = mfma(xa[u], qw[u], qa); }
    *(f32x4*)(lds + L_QRED + w * 256 + (lane >> 5) * 128 + 8 * c + 4 * (r4 & 1)) = qa;
  }
  asm volatile("" :: "v"(epoch));
  if (tid == 0) ldsu[L_TAG] = epoch + 1u;
  STAMP(1);
  LDS_BARRIER();
  STAMP(2);
  const u32 tag = __builtin_amdgcn_readfirstlane(ldsu[L_TAG]);
  if (w < 2) {
    f32x2 sum = {0, 0};
#pragma unroll
    for (int ww = 0; ww < 8; ww++) sum += *(const f32x2*)(lds + L_QRED + ww * 256 + tid * 2);
    ST_SC1(mk_unit(sum[0], sum[1], tag), rws, tid * 16, WS_QP + (h * 8 + j) * QP_STRIDE);
  }
  u32x4 qg[8];
  {
    const rsrc_t rq = mk_rsrc(ws, w == 0 ? WS_END : 0);
    if (w == 0 && D2 > 0) __builtin_amdgcn_s_sleep(D2);
#pragma unroll
    for (int p = 0; p < 8; p++) qg[p] = LD_SC1(rq, lane * 16 + b * 1024, WS_QP + (h * 8 + p) * QP_STRIDE);
  }
  RAW_BARRIER();
  const int kvoff = lane * 16;
  const int rotc = ROT_EXPR;
  constexpr int NSMALL = 64 - WSK;
  const int nseq = w < 4 ? 64 + WSK : NSMALL;
#define SLOT_OF(i) ((i) < NSMALL ? w + 8 * (i) : 8 * NSMALL + w + 4 * ((i) - NSMALL))
#define SLOT_OFF(t) (((4 * ((t) >> 4) + s + rotc) & 127) * 16384 + ((t) & 15) * 1024)
  u32x4 kr[2], vr[2];
  kr[0] = LD_NT(rk, kvoff, SLOT_OFF(SLOT_OF(0))); vr[0] = LD_NT(rv, kvoff, SLOT_OFF(SLOT_OF(0)));
  u32x4 gx0, gx1, gg0, gg1;
  {
    const rsrc_t rxb = mk_rsrc((const unsigned char*)x + (w & 1) * D * 2, w >= 6 ? D * 2 : 0), rgw = mk_rsrc(gw, w >= 6 ? D * 2 : 0);
    gx0 = __builtin_amdgcn_raw_buffer_load_b128(rxb, lane * 16, 0, 0); gx1 = __builtin_amdgcn_raw_buffer_load_b128(rxb, lane * 16 + 1024, 0, 0);
    gg0 = __builtin_amdgcn_raw_buffer_load_b128(rgw, lane * 16, 0, 0); gg1 = __builtin_amdgcn_raw_buffer_load_b128(rgw, lane * 16 + 1024, 0, 0);
  }
  __builtin_amdgcn_sched_barrier(0);
  STAMP(3);
  {
    f32x4 fa = {0, 0, 0, 0};
#pragma unroll
    for (int u = 0; u < 3; u++) fa = mfma(xa[u], fw[u], fa);
    *(f32x4*)(lds + L_FRED + w * 256 + (lane >> 5) * 128 + 8 * c + 4 * (r4 & 1)) = fa;
  }
  asm volatile("" :: "v"(qg[0]), "v"(qg[1]), "v"(qg[2]), "v"(qg[3]), "v"(qg[4]), "v"(qg[5]), "v"(qg[6]), "v"(qg[7]));
  if (w == 0) {
    unsigned spins = 0;
    bool ok = true;
    float q0 = 0.f, q1 = 0.f;
#pragma unroll
    for (int p = 0; p < 8; p++) { ok &= unit_ok(qg[p], tag); q0 += __uint_as_float(qg[p][0]); q1 += __uint_as_float(qg[p][2]); }
    if (!__all(ok)) {
      do {
        __builtin_amdgcn_s_sleep(1);
        int vo = lane * 16 + b * 1024;
        asm volatile("" : "+v"(vo) :: "memory");
        ok = true; q0 = 0.f; q1 = 0.f;
#pragma unroll
        for (int p = 0; p < 8; p++) qg[p] = LD_SC1(rws, vo, WS_QP + (h * 8 + p) * QP_STRIDE);
#pragma unroll
        for (int p = 0; p < 8; p++) { ok &= unit_ok(qg[p], tag); q0 += __uint_as_float(qg[p][0]); q1 += __uint_as_float(qg[p][2]); }
      } while (!__all(ok) && ++spins < SPIN_CAP);
      spins++;
    }
    ldsu[L_Q + lane] = pack_bf(q0, q1);
  }
  STAMP(4);
  LDS_BARRIER();
  const u32x4 qp = *(const u32x4*)(ldsu + L_Q + c * 4);
  asm volatile("" :: "v"(qp));
  STAMP(5);

  constexpr float C2 = 0.08838834764831845f * 1.4426950408889634f;
  float mraw = -INFINITY, mc = -INFINITY, l = 0.f;
  f32x4 acc = {0, 0, 0, 0};
  int it = 0;
#define STEP(i) do { \
      const int so_ = it + 1 < nseq ? SLOT_OFF(SLOT_OF(it + 1)) : OOB; \
      kr[(i) ^ 1] = LD_NT(rk, kvoff, so_); vr[(i) ^ 1] = LD_NT(rv, kvoff, so_); \
      __builtin_amdgcn_sched_barrier(0); \
      if (it >= nseq) goto loop_done; \
      float sc = dot2(kr[i][0], qp[0], 0.f); sc = dot2(kr[i][1], qp[1], sc); sc = dot2(kr[i][2], qp[2], sc); sc = dot2(kr[i][3], qp[3], sc); \
      sc = row16_sum(sc); \
      if (__builtin_amdgcn_ballot_w64(sc > mraw) != 0) {        \
        const float mn = xrow_max(fmaxf(sc, mraw)); \
        const float mnc = mn * C2; \
        const float al = __builtin_amdgcn_exp2f(mc - mnc); \
        acc *= al; l *= al; mraw = mn; mc = mnc; \
      } \
      const float p = __builtin_amdgcn_exp2f(fmaf(sc, C2, -mc)); \
      l += p; \
      u32 pp = pack_bf(p, p); \
      if (HILO) { const float pl = p - bflo(pp); const u32 pq = pack_bf(pl, pl); pp = (lane & 8) ? pq : pp; }     \
      acc = mfma(onehot(pp, msk), vr[i], acc); \
      asm volatile("" : "+v"(acc), "+v"(l)); \
      __builtin_amdgcn_sched_barrier(0); \
      it++; } while (0)
  if (w == 2 || w == 3) {
    float g = 0.f, u = 0.f;
#pragma unroll
    for (int ww = 0; ww < 8; ww++) { g += lds[L_FRED + ww * 256 + (w - 2) * 128 + lane]; u += lds[L_FRED + ww * 256 + (w - 2) * 128 + 64 + lane]; }
    if (has_ffn) ST_SC1(mk_unit(g, u, tag), rws, (tid - 128) * 16, WS_FFN + (h * 8 + j) * FFN_STRIDE);
  }
  for (int rd = 0; rd < WT_AT; rd++) { STEP(0); STEP(1); }
  u32x4 wt[6];
  {
    const int wtoff = c < 10 ? (r4 * D + 8 * c) * 2 : OOB;
    const rsrc_t rwo = mk_rsrc(wo, 4096 * D * 2), rwd = mk_rsrc(wd, has_ffn ? F * D * 2 : 0);
#pragma unroll
    for (int i = 0; i < 4; i++) wt[i] = LD_NT(rwo, wtoff, ((128 * h + 16 * w + 4 * i) * D + 80 * j) * 2);
#pragma unroll
    for (int i = 0; i < 2; i++) wt[4 + i] = LD_NT(rwd, wtoff, ((64 * fb + 8 * w + 4 * i) * D + 80 * j) * 2);
    __builtin_amdgcn_sched_barrier(0);
  }
  asm volatile("" :: "v"(gx0), "v"(gx1), "v"(gg0), "v"(gg1));
  if (w >= 6) {
    float gs = 0.f;
#pragma unroll
    for (int k = 0; k < 4; k++) {
      gs += rbf(bflo(gx0[k]) * bflo(gg0[k])) + rbf(bfhi(gx0[k]) * bfhi(gg0[k]));
      gs += rbf(bflo(gx1[k]) * bflo(gg1[k])) + rbf(bfhi(gx1[k]) * bfhi(gg1[k]));
    }
    gs = xrow_sum(row16_sum(gs));
    const float g = rbf(1.f / (1.f + __expf(-rbf(gs))));
    if (lane == 0) { lds[L_G + w - 6] = g; lds[L_G + 2 + w - 6] = rbf(1.f - g); }
  }
  for (;;) { STEP(0); STEP(1); }
loop_done:
  STAMP(6);

  l = xrow_sum(l);
  if (HILO) {
#pragma unroll
    for (int k = 0; k < 4; k++) acc[k] = xhalf_sum(acc[k]);
  }
  if (lane < 32) *(f32x4*)(lds + L_PART + w * 132 + 8 * c + 4 * r4) = acc;
  if (lane == 0) *(f32x2*)(lds + L_PART + w * 132 + 128) = f32x2{mc, l};
  LDS_BARRIER();
  STAMP(7);
  if (w == 0) {
    float mw[8], lw[8], M = -INFINITY;
    f32x2 a[8];
#pragma unroll
    for (int ww = 0; ww < 8; ww++) { const f32x2 t = *(const f32x2*)(lds + L_PART + ww * 132 + 128); mw[ww] = t[0]; lw[ww] = t[1]; a[ww] = *(const f32x2*)(lds + L_PART + ww * 132 + lane * 2); }
#pragma unroll
    for (int ww = 0; ww < 8; ww++) M = fmaxf(M, mw[ww]);
    float L = 0.f, o0 = 0.f, o1 = 0.f;
#pragma unroll
    for (int ww = 0; ww < 8; ww++) {
      const float e = __builtin_amdgcn_exp2f(mw[ww] - M);
      L = fmaf(e, lw[ww], L); o0 = fmaf(e, a[ww][0], o0); o1 = fmaf(e, a[ww][1], o1);
    }
    const int so = WS_HOP1 + (h * 8 + j) * HOP1_STRIDE;
    ST_SC1(mk_unit(o0, o1, tag), rws, lane * 16, so);
    if (lane == 0) ST_SC1(mk_unit(M, L, tag), rws, 1024, so);
  } else if (w <= 2) {
    const int bb = w - 1;
    u32x4 a[8];
    poll_units<8>(a, tag, [&](u32x4 (&r)[8]) {
      int vo = lane * 16, vz = 1024;
      asm volatile("" : "+v"(vo), "+v"(vz) :: "memory");
#pragma unroll
      for (int p = 0; p < 4; p++) { const int so = WS_HOP1 + (h * 8 + bb * 4 + p) * HOP1_STRIDE; r[p] = LD_SC1(rws, vo, so); r[4 + p] = LD_SC1(rws, vz, so); }
    });
    float M = -INFINITY;
#pragma unroll
    for (int p = 0; p < 4; p++) M = fmaxf(M, __uint_as_float(a[4 + p][0]));
    float L = 0.f, o0 = 0.f, o1 = 0.f;
#pragma unroll
    for (int p = 0; p < 4; p++) {
      const float e = __builtin_amdgcn_exp2f(__uint_as_float(a[4 + p][0]) - M);
      L = fmaf(e, __uint_as_float(a[4 + p][2]), L); o0 = fmaf(e, __uint_as_float(a[p][0]), o0); o1 = fmaf(e, __uint_as_float(a[p][2]), o1);
    }
    const float inv = __builtin_amdgcn_rcpf(L);
    o0 *= inv; o1 *= inv;
    *(u32x2*)(ldsu + L_VV + bb * 192 + lane * 2) = u32x2{pack_bf(o0, o0), pack_bf(o1, o1)};
  } else if (w <= 4) {
    const int bb = w - 3;
    float g = 0.f, u = 0.f;
    if (has_ffn) {
      u32x4 a[8];
      poll_units<8>(a, tag, [&](u32x4 (&r)[8]) {
        int vo = lane * 16 + bb * 1024;
        asm volatile("" : "+v"(vo) :: "memory");
#pragma unroll
        for (int p = 0; p < 8; p++) r[p] = LD_SC1(rws, vo, WS_FFN + (h * 8 + p) * FFN_STRIDE);
      });
#pragma unroll
      for (int p = 0; p < 8; p++) { g += __uint_as_float(a[p][0]); u += __uint_as_float(a[p][2]); }
    }
    g = rbf(g); u = rbf(u);
    const float t = 0.7978845608028654f * (g + 0.044715f * g * g * g);
    const float th = 1.f - 2.f / (1.f + __expf(2.f * t));
    const float hid = rbf(0.5f * g * (1.f + th)) * u;
    ldsu[L_VV + bb * 192 + 128 + lane] = pack_bf(hid, hid);
  }
  LDS_BARRIER();
  STAMP(8);
  {
    f32x4 ao0 = {0, 0, 0, 0}, ao1 = {0, 0, 0, 0}, ad = {0, 0, 0, 0};
    const u32* vv = ldsu + L_VV + (c >> 3) * 192;
    const int bb = lane >> 5;
    const float g = lds[L_G + bb], g1 = lds[L_G + 2 + bb];
    ao0 = mfma(onehot(vv[16 * w + r4], msk), wt[0], ao0);
    ao1 = mfma(onehot(vv[16 * w + 4 + r4], msk), wt[1], ao1);
    ad = mfma(onehot(vv[128 + 8 * w + r4], msk), wt[4], ad);
    ao0 = mfma(onehot(vv[16 * w + 8 + r4], msk), wt[2], ao0);
    ao1 = mfma(onehot(vv[16 * w + 12 + r4], msk), wt[3], ao1);
    ad = mfma(onehot(vv[128 + 8 * w + 4 + r4], msk), wt[5], ad);
    if (c < 10) *(f32x4*)(lds + L_RED + w * 160 + bb * 80 + 8 * c + 4 * (r4 & 1)) = g * (ao0 + ao1) + g1 * ad;
  }
  LDS_BARRIER();
  STAMP(9);
  if (tid < 80) {
    f32x2 sum = {0, 0};
#pragma unroll
    for (int ww = 0; ww < 8; ww++) sum += *(const f32x2*)(lds + L_RED + ww * 160 + tid * 2);
    ST_SC1(mk_unit(sum[0], sum[1], tag), rws, tid * 16, WS_HOP2 + (j * 32 + h) * HOP2_STRIDE);
  }
  if (w == 2 && h < 20) {
    const int bb = h / 10, cg = h % 10;
    const int hp = lane >> 1, half = lane & 1;
    const int vo0 = hp * HOP2_STRIDE + (bb * 40 + cg * 4 + half * 2) * 16;
    const int so = WS_HOP2 + j * 32 * HOP2_STRIDE;
    u32x4 a[2];
    poll_units<2>(a, tag, [&](u32x4 (&r)[2]) {
      int vv = vo0;
      asm volatile("" : "+v"(vv) :: "memory");
      r[0] = LD_SC1(rws, vv, so); r[1] = LD_SC1(rws, vv + 16, so);
    });
    float v[4] = {__uint_as_float(a[0][0]), __uint_as_float(a[0][2]), __uint_as_float(a[1][0]), __uint_as_float(a[1][2])};
#pragma unroll
    for (int k = 0; k < 4; k++) {
      v[k] += dpp<0x4E>(v[k]);
      v[k] += dpp<0x124>(v[k]);
      v[k] += dpp<0x128>(v[k]);
      v[k] = xrow_sum(v[k]);
    }
    if (lane < 2) {
      const u32x2 r = {pack_bf(v[0], v[1]), pack_bf(v[2], v[3])};
      __builtin_amdgcn_raw_buffer_store_b64(r, mk_rsrc(out, 2 * D * 2), (bb * 640 + 80 * j + 8 * cg + 4 * half) * 2, 0, OUT_AUX);
    }
    if (blk == 0 && lane == 0) __builtin_amdgcn_raw_buffer_store_b32(tag, rws, 0, WS_EPOCH, AUX_SC1);
  }
}

extern "C" void kernel_launch(void* const* d_in, const int* in_sizes, int n_in, void* d_out, int out_size, void* d_ws, size_t ws_size, hipStream_t stream) {
  fused_decode<<<NWG, NTHR, 0, stream>>>((const u32*)d_in[0], (const u32*)d_in[1], (const u32*)d_in[2], (const u32*)d_in[3], (const u32*)d_in[4],
                                         (const u32*)d_in[6], (const u32*)d_in[7], (const u32*)d_in[8], (const u32*)d_in[9], (u32*)d_out, (unsigned char*)d_ws);
}
```

```cpp
#include <hip/hip_runtime.h>
#include <hip/hip_bf16.h>
#include <stdint.h>
#define ENC0 0
#define AUX_KV 3
#define OUT_AUX 16

typedef unsigned u32;
using u32x4 = __attribute__((ext_vector_type(4))) unsigned;
using u32x2 = __attribute__((ext_vector_type(2))) unsigned;
using f32x4 = __attribute__((ext_vector_type(4))) float;
using f32x2 = __attribute__((ext_vector_type(2))) float;
using bf16x2 = __attribute__((ext_vector_type(2))) __bf16;
using bf16x8 = __attribute__((ext_vector_type(8))) __bf16;
typedef __amdgpu_buffer_rsrc_t rsrc_t;
#define GAS __attribute__((address_space(1)))

#define D1 12
#define D2 24
#define WT_AT 2
#define WSK 1
#define ROT_EXPR (12 * b)
#define RSC 1
#define HILO 1

constexpr int D = 640, SK = 8192, DH = 128, F = 1728;
constexpr int NWG = 256, NTHR = 512;
constexpr int AUX_NT = 2, AUX_SC1 = 16;
constexpr int OOB = (int)0x80000000;

constexpr unsigned WS_EPOCH = 0;
constexpr unsigned WS_QP = 4096, QP_STRIDE = 2048;
constexpr unsigned WS_FFN = WS_QP + 256 * QP_STRIDE, FFN_STRIDE = 2048;
constexpr unsigned WS_HOP1 = WS_FFN + 256 * FFN_STRIDE, HOP1_STRIDE = 1152;
constexpr unsigned WS_HOP2 = WS_HOP1 + 256 * HOP1_STRIDE, HOP2_STRIDE = 1280;
constexpr unsigned WS_END = WS_HOP2 + 256 * HOP2_STRIDE;

constexpr int L_QRED = 0;
constexpr int L_FRED = L_QRED + 2048;
constexpr int L_Q = L_FRED + 2048;
constexpr int L_PART = L_Q + 64;
constexpr int L_VV = L_PART + 8 * 132;
constexpr int L_RED = L_VV + 384;
constexpr int L_G = L_RED + 1280;
constexpr int L_TAG = L_G + 4;
constexpr int L_END = L_TAG + 4;

__device__ __forceinline__ float bflo(u32 u) { return __builtin_bit_cast(float, u << 16); }
__device__ __forceinline__ float bfhi(u32 u) { return __builtin_bit_cast(float, u & 0xffff0000u); }
__device__ __forceinline__ float rbf(float x) { return (float)(__bf16)x; }
__device__ __forceinline__ u32 pack_bf(float lo, float hi) {
  bf16x2 v = {(__bf16)lo, (__bf16)hi};
  return __builtin_bit_cast(u32, v);
}
__device__ __forceinline__ float dot2(u32 a, u32 b, float c) {
  return __builtin_amdgcn_fdot2_f32_bf16(__builtin_bit_cast(bf16x2, a), __builtin_bit_cast(bf16x2, b), c, false);
}
__device__ __forceinline__ f32x4 mfma(u32x4 a, u32x4 b, f32x4 c) {
  return __builtin_amdgcn_mfma_f32_16x16x32_bf16(__builtin_bit_cast(bf16x8, a), __builtin_bit_cast(bf16x8, b), c, 0, 0, 0);
}
template <int CTRL> __device__ __forceinline__ float dpp(float x) {
  return __builtin_bit_cast(float, __builtin_amdgcn_mov_dpp(__builtin_bit_cast(int, x), CTRL, 0xf, 0xf, true));
}
__device__ __forceinline__ float row16_sum(float x) {
  x += dpp<0xB1>(x);
  x += dpp<0x4E>(x);
  x += dpp<0x141>(x);
  x += dpp<0x140>(x);
  return x;
}
__device__ __forceinline__ float xhalf_sum(float x) {
  auto t = __builtin_amdgcn_permlane32_swap(__float_as_uint(x), __float_as_uint(x), false, false);
  return __uint_as_float(t[0]) + __uint_as_float(t[1]);
}
__device__ __forceinline__ float xrow_sum(float x) {
  auto s = __builtin_amdgcn_permlane16_swap(__float_as_uint(x), __float_as_uint(x), false, false);
  return xhalf_sum(__uint_as_float(s[0]) + __uint_as_float(s[1]));
}
__device__ __forceinline__ float xrow_max(float x) {
  auto s = __builtin_amdgcn_permlane16_swap(__float_as_uint(x), __float_as_uint(x), false, false);
  x = fmaxf(__uint_as_float(s[0]), __uint_as_float(s[1]));
  auto t = __builtin_amdgcn_permlane32_swap(__float_as_uint(x), __float_as_uint(x), false, false);
  return fmaxf(__uint_as_float(t[0]), __uint_as_float(t[1]));
}
#define LD_NT(r, voff, soff) __builtin_amdgcn_raw_buffer_load_b128((r), (voff), (soff), AUX_NT)
#define LD_KV(r, voff, soff) __builtin_amdgcn_raw_buffer_load_b128((r), (voff), (soff), AUX_KV)
#define LD_SC1(r, voff, soff) __builtin_amdgcn_raw_buffer_load_b128((r), (voff), (soff), AUX_SC1)
#define ST_SC1(v, r, voff, soff) __builtin_amdgcn_raw_buffer_store_b128((v), (r), (voff), (soff), AUX_SC1)
#define LDS_BARRIER() do { asm volatile("s_waitcnt lgkmcnt(0)" ::: "memory"); __builtin_amdgcn_s_barrier(); asm volatile("" ::: "memory"); } while (0)
#define RAW_BARRIER() do { __builtin_amdgcn_sched_barrier(0); __builtin_amdgcn_s_barrier(); __builtin_amdgcn_sched_barrier(0); } while (0)
__device__ __forceinline__ rsrc_t mk_rsrc(const void* p, unsigned bytes) { return __builtin_amdgcn_make_buffer_rsrc((void*)p, 0, bytes, 0x00020000); }
__device__ __forceinline__ u32x4 mk_unit(float a, float b, u32 tag) { u32x4 v = {__float_as_uint(a), tag, __float_as_uint(b), tag}; return v; }
__device__ __forceinline__ bool unit_ok(u32x4 v, u32 tag) { return (v[1] == tag) & (v[3] == tag); }
__device__ __forceinline__ u32x4 onehot(u32 dup, const u32 (&msk)[4]) { u32x4 a = {dup & msk[0], dup & msk[1], dup & msk[2], dup & msk[3]}; return a; }

constexpr unsigned SPIN_CAP = 1u << 18;
#define STAMP(k) do { } while (0)
#define STAMP_DECL do { } while (0)
#define STAMP_FLUSH() do { } while (0)

template <int N, typename Issue>
__device__ __forceinline__ void poll_units(u32x4 (&u)[N], u32 tag, Issue issue) {
  for (unsigned spins = 0;;) {
    issue(u);
    bool ok = true;
#pragma unroll
    for (int k = 0; k < N; k++) ok &= unit_ok(u[k], tag);
    if (__all(ok) || ++spins > SPIN_CAP) break;
    __builtin_amdgcn_s_sleep(1);
  }
}

__global__ void __launch_bounds__(NTHR, 2)
fused_decode(const u32* __restrict__ x, const u32* __restrict__ kc, const u32* __restrict__ vc, const u32* __restrict__ wq,
             const u32* __restrict__ wo, const u32* __restrict__ wg, const u32* __restrict__ wu, const u32* __restrict__ wd,
             const u32* __restrict__ gw, u32* __restrict__ out, unsigned char* __restrict__ ws) {
  __shared__ __attribute__((aligned(16))) float lds[L_END];
  u32* const ldsu = (u32*)lds;
  const int tid = threadIdx.x, lane = tid & 63, w = __builtin_amdgcn_readfirstlane(tid >> 6);
  const int r4 = lane >> 4, c = lane & 15;
  const int blk = blockIdx.x, xg = blk & 7, li = blk >> 3;
  const int h = xg * 4 + (li >> 3), j = li & 7, b = j >> 2, s = j & 3;
  const int fb = (h & 3) * 8 + (h >> 2);
  const bool has_ffn = fb < 27;
  const int bh = b * 32 + h;
  STAMP_DECL;
  STAMP(0);
  const unsigned long long enc_t0 = __builtin_amdgcn_s_memrealtime();

  const rsrc_t rws = mk_rsrc(ws, WS_END);
  const rsrc_t rk = mk_rsrc((const unsigned char*)kc + (size_t)bh * (SK * DH * 2), SK * DH * 2);
  const rsrc_t rv = mk_rsrc((const unsigned char*)vc + (size_t)bh * (SK * DH * 2), SK * DH * 2);

  u32 xs[3], epoch;
  u32x4 qw[3], fw[3];
  const int row0 = 80 * j + 10 * w + r4;
  {
    const rsrc_t rx = mk_rsrc(x, 2 * D * 2);
    const rsrc_t rwq = mk_rsrc(wq, D * 4096 * 2);
#pragma unroll
    for (int u = 0; u < 3; u++) {
      const bool ok = (u < 2) || (r4 < 2);
      xs[u] = __builtin_amdgcn_raw_buffer_load_b16(rx, ok ? ((c >> 3) * D + row0 + 4 * u) * 2 : OOB, 0, 0);
    }
#pragma unroll
    for (int u = 0; u < 3; u++) {
      const bool ok = (u < 2) || (r4 < 2);
      qw[u] = LD_NT(rwq, ok ? ((row0 + 4 * u) * 4096 + 128 * h + 8 * c) * 2 : OOB, 0);
    }
    epoch = __builtin_amdgcn_raw_buffer_load_b32(rws, tid == 0 ? 0 : OOB, WS_EPOCH, AUX_SC1);
  }
  __builtin_amdgcn_sched_barrier(0);
  if (D1 > 0) __builtin_amdgcn_s_sleep(D1);
  RAW_BARRIER();
  {
    const int fbc = has_ffn ? fb : 0;
    const GAS unsigned char* fbase = (const GAS unsigned char*)((c < 8) ? wg : wu) + (64 * fbc + 8 * (c & 7)) * 2;
#pragma unroll
    for (int u = 0; u < 3; u++) {
      const int row = (u < 2 || r4 < 2) ? row0 + 4 * u : row0;
      fw[u] = __builtin_nontemporal_load((const GAS u32x4*)(fbase + (size_t)row * (F * 2)));
    }
  }
  __builtin_amdgcn_sched_barrier(0);
  u32 msk[4];
#pragma unroll
  for (int k = 0; k < 4; k++) msk[k] = (k == ((lane & 7) >> 1)) ? ((lane & 1) ? 0xffff0000u : 0x0000ffffu) : 0u;
  u32x4 xa[3];
  {
    f32x4 qa = {0, 0, 0, 0};
#pragma unroll
    for (int u = 0; u < 3; u++) { xa[u] = onehot(xs[u] * 0x10001u, msk); qa = mfma(xa[u], qw[u], qa); }
    *(f32x4*)(lds + L_QRED + w * 256 + (lane >> 5) * 128 + 8 * c + 4 * (r4 & 1)) = qa;
  }
  asm volatile("" :: "v"(epoch));
  if (tid == 0) ldsu[L_TAG] = epoch + 1u;
  STAMP(1);
  LDS_BARRIER();
  STAMP(2);
  const u32 tag = __builtin_amdgcn_readfirstlane(ldsu[L_TAG]);
  if (w < 2) {
    f32x2 sum = {0, 0};
#pragma unroll
    for (int ww = 0; ww < 8; ww++) sum += *(const f32x2*)(lds + L_QRED + ww * 256 + tid * 2);
    ST_SC1(mk_unit(sum[0], sum[1], tag), rws, tid * 16, WS_QP + (h * 8 + j) * QP_STRIDE);
  }
  u32x4 qg[8];
  {
    const rsrc_t rq = mk_rsrc(ws, w == 0 ? WS_END : 0);
    if (w == 0 && D2 > 0) __builtin_amdgcn_s_sleep(D2);
#pragma unroll
    for (int p = 0; p < 8; p++) qg[p] = LD_SC1(rq, lane * 16 + b * 1024, WS_QP + (h * 8 + p) * QP_STRIDE);
  }
  RAW_BARRIER();
  const int kvoff = lane * 16;
  const int rotc = ROT_EXPR;
  constexpr int NSMALL = 64 - WSK;
  const int nseq = w < 4 ? 64 + WSK : NSMALL;
#define SLOT_OF(i) ((i) < NSMALL ? w + 8 * (i) : 8 * NSMALL + w + 4 * ((i) - NSMALL))
#define SLOT_OFF(t) (((4 * ((t) >> 4) + s + rotc) & 127) * 16384 + ((t) & 15) * 1024)
  u32x4 kr[2], vr[2];
  kr[0] = LD_KV(rk, kvoff, SLOT_OFF(SLOT_OF(0))); vr[0] = LD_KV(rv, kvoff, SLOT_OFF(SLOT_OF(0)));
  u32x4 gx0, gx1, gg0, gg1;
  {
    const rsrc_t rxb = mk_rsrc((const unsigned char*)x + (w & 1) * D * 2, w >= 6 ? D * 2 : 0), rgw = mk_rsrc(gw, w >= 6 ? D * 2 : 0);
    gx0 = __builtin_amdgcn_raw_buffer_load_b128(rxb, lane * 16, 0, 0); gx1 = __builtin_amdgcn_raw_buffer_load_b128(rxb, lane * 16 + 1024, 0, 0);
    gg0 = __builtin_amdgcn_raw_buffer_load_b128(rgw, lane * 16, 0, 0); gg1 = __builtin_amdgcn_raw_buffer_load_b128(rgw, lane * 16 + 1024, 0, 0);
  }
  __builtin_amdgcn_sched_barrier(0);
  STAMP(3);
  {
    f32x4 fa = {0, 0, 0, 0};
#pragma unroll
    for (int u = 0; u < 3; u++) fa = mfma(xa[u], fw[u], fa);
    *(f32x4*)(lds + L_FRED + w * 256 + (lane >> 5) * 128 + 8 * c + 4 * (r4 & 1)) = fa;
  }
  asm volatile("" :: "v"(qg[0]), "v"(qg[1]), "v"(qg[2]), "v"(qg[3]), "v"(qg[4]), "v"(qg[5]), "v"(qg[6]), "v"(qg[7]));
  if (w == 0) {
    unsigned spins = 0;
    bool ok = true;
    float q0 = 0.f, q1 = 0.f;
#pragma unroll
    for (int p = 0; p < 8; p++) { ok &= unit_ok(qg[p], tag); q0 += __uint_as_float(qg[p][0]); q1 += __uint_as_float(qg[p][2]); }
    if (!__all(ok)) {
      do {
        __builtin_amdgcn_s_sleep(1);
        int vo = lane * 16 + b * 1024;
        asm volatile("" : "+v"(vo) :: "memory");
        ok = true; q0 = 0.f; q1 = 0.f;
#pragma unroll
        for (int p = 0; p < 8; p++) qg[p] = LD_SC1(rws, vo, WS_QP + (h * 8 + p) * QP_STRIDE);
#pragma unroll
        for (int p = 0; p < 8; p++) { ok &= unit_ok(qg[p], tag); q0 += __uint_as_float(qg[p][0]); q1 += __uint_as_float(qg[p][2]); }
      } while (!__all(ok) && ++spins < SPIN_CAP);
      spins++;
    }
    ldsu[L_Q + lane] = pack_bf(q0, q1);
  }
  STAMP(4);
  LDS_BARRIER();
  const u32x4 qp = *(const u32x4*)(ldsu + L_Q + c * 4);
  asm volatile("" :: "v"(qp));
  STAMP(5);

  constexpr float C2 = (RSC ? 1.f : 0.08838834764831845f) * 1.4426950408889634f;
  float mraw = -INFINITY, mc = -INFINITY, l = 0.f;
  f32x4 acc = {0, 0, 0, 0};
  int it = 0;
#define STEP(i) do { \
      const int so_ = it + 1 < nseq ? SLOT_OFF(SLOT_OF(it + 1)) : OOB; \
      kr[(i) ^ 1] = LD_KV(rk, kvoff, so_); vr[(i) ^ 1] = LD_KV(rv, kvoff, so_); \
      __builtin_amdgcn_sched_barrier(0); \
      if (it >= nseq) goto loop_done; \
      float sc = dot2(kr[i][0], qp[0], 0.f); sc = dot2(kr[i][1], qp[1], sc); sc = dot2(kr[i][2], qp[2], sc); sc = dot2(kr[i][3], qp[3], sc); \
      sc = row16_sum(sc); \
      if (RSC) sc = rbf(rbf(sc) * 0.08838834764831845f); \
      if (__builtin_amdgcn_ballot_w64(sc > mraw) != 0) {        \
        const float mn = xrow_max(fmaxf(sc, mraw)); \
        const float mnc = mn * C2; \
        const float al = __builtin_amdgcn_exp2f(mc - mnc); \
        acc *= al; l *= al; mraw = mn; mc = mnc; \
      } \
      const float p = __builtin_amdgcn_exp2f(fmaf(sc, C2, -mc)); \
      l += p; \
      u32 pp = pack_bf(p, p); \
      if (HILO) { const float pl = p - bflo(pp); const u32 pq = pack_bf(pl, pl); pp = (lane & 8) ? pq : pp; }     \
      acc = mfma(onehot(pp, msk), vr[i], acc); \
      asm volatile("" : "+v"(acc), "+v"(l)); \
      __builtin_amdgcn_sched_barrier(0); \
      it++; } while (0)
  if (w == 2 || w == 3) {
    float g = 0.f, u = 0.f;
#pragma unroll
    for (int ww = 0; ww < 8; ww++) { g += lds[L_FRED + ww * 256 + (w - 2) * 128 + lane]; u += lds[L_FRED + ww * 256 + (w - 2) * 128 + 64 + lane]; }
    if (has_ffn) ST_SC1(mk_unit(g, u, tag), rws, (tid - 128) * 16, WS_FFN + (h * 8 + j) * FFN_STRIDE);
  }
  for (int rd = 0; rd < WT_AT; rd++) { STEP(0); STEP(1); }
  u32x4 wt[6];
  {
    const int wtoff = c < 10 ? (r4 * D + 8 * c) * 2 : OOB;
    const rsrc_t rwo = mk_rsrc(wo, 4096 * D * 2), rwd = mk_rsrc(wd, has_ffn ? F * D * 2 : 0);
#pragma unroll
    for (int i = 0; i < 4; i++) wt[i] = LD_NT(rwo, wtoff, ((128 * h + 16 * w + 4 * i) * D + 80 * j) * 2);
#pragma unroll
    for (int i = 0; i < 2; i++) wt[4 + i] = LD_NT(rwd, wtoff, ((64 * fb + 8 * w + 4 * i) * D + 80 * j) * 2);
    __builtin_amdgcn_sched_barrier(0);
  }
  asm volatile("" :: "v"(gx0), "v"(gx1), "v"(gg0), "v"(gg1));
  if (w >= 6) {
    float gs = 0.f;
#pragma unroll
    for (int k = 0; k < 4; k++) {
      gs += rbf(bflo(gx0[k]) * bflo(gg0[k])) + rbf(bfhi(gx0[k]) * bfhi(gg0[k]));
      gs += rbf(bflo(gx1[k]) * bflo(gg1[k])) + rbf(bfhi(gx1[k]) * bfhi(gg1[k]));
    }
    gs = xrow_sum(row16_sum(gs));
    const float g = rbf(1.f / (1.f + __expf(-rbf(gs))));
    if (lane == 0) { lds[L_G + w - 6] = g; lds[L_G + 2 + w - 6] = rbf(1.f - g); }
  }
  for (;;) { STEP(0); STEP(1); }
loop_done:
  STAMP(6);

  l = xrow_sum(l);
  if (HILO) {
#pragma unroll
    for (int k = 0; k < 4; k++) acc[k] = xhalf_sum(acc[k]);
  }
  if (lane < 32) *(f32x4*)(lds + L_PART + w * 132 + 8 * c + 4 * r4) = acc;
  if (lane == 0) *(f32x2*)(lds + L_PART + w * 132 + 128) = f32x2{mc, l};
  LDS_BARRIER();
  STAMP(7);
  if (w == 0) {
    float mw[8], lw[8], M = -INFINITY;
    f32x2 a[8];
#pragma unroll
    for (int ww = 0; ww < 8; ww++) { const f32x2 t = *(const f32x2*)(lds + L_PART + ww * 132 + 128); mw[ww] = t[0]; lw[ww] = t[1]; a[ww] = *(const f32x2*)(lds + L_PART + ww * 132 + lane * 2); }
#pragma unroll
    for (int ww = 0; ww < 8; ww++) M = fmaxf(M, mw[ww]);
    float L = 0.f, o0 = 0.f, o1 = 0.f;
#pragma unroll
    for (int ww = 0; ww < 8; ww++) {
      const float e = __builtin_amdgcn_exp2f(mw[ww] - M);
      L = fmaf(e, lw[ww], L); o0 = fmaf(e, a[ww][0], o0); o1 = fmaf(e, a[ww][1], o1);
    }
    const int so = WS_HOP1 + (h * 8 + j) * HOP1_STRIDE;
    ST_SC1(mk_unit(o0, o1, tag), rws, lane * 16, so);
    if (lane == 0) ST_SC1(mk_unit(M, L, tag), rws, 1024, so);
  } else if (w <= 2) {
    const int bb = w - 1;
    u32x4 a[8];
    poll_units<8>(a, tag, [&](u32x4 (&r)[8]) {
      int vo = lane * 16, vz = 1024;
      asm volatile("" : "+v"(vo), "+v"(vz) :: "memory");
#pragma unroll
      for (int p = 0; p < 4; p++) { const int so = WS_HOP1 + (h * 8 + bb * 4 + p) * HOP1_STRIDE; r[p] = LD_SC1(rws, vo, so); r[4 + p] = LD_SC1(rws, vz, so); }
    });
    float M = -INFINITY;
#pragma unroll
    for (int p = 0; p < 4; p++) M = fmaxf(M, __uint_as_float(a[4 + p][0]));
    float L = 0.f, o0 = 0.f, o1 = 0.f;
#pragma unroll
    for (int p = 0; p < 4; p++) {
      const float e = __builtin_amdgcn_exp2f(__uint_as_float(a[4 + p][0]) - M);
      L = fmaf(e, __uint_as_float(a[4 + p][2]), L); o0 = fmaf(e, __uint_as_float(a[p][0]), o0); o1 = fmaf(e, __uint_as_float(a[p][2]), o1);
    }
    const float inv = __builtin_amdgcn_rcpf(L);
    o0 *= inv; o1 *= inv;
    *(u32x2*)(ldsu + L_VV + bb * 192 + lane * 2) = u32x2{pack_bf(o0, o0), pack_bf(o1, o1)};
  } else if (w <= 4) {
    const int bb = w - 3;
    float g = 0.f, u = 0.f;
    if (has_ffn) {
      u32x4 a[8];
      poll_units<8>(a, tag, [&](u32x4 (&r)[8]) {
        int vo = lane * 16 + bb * 1024;
        asm volatile("" : "+v"(vo) :: "memory");
#pragma unroll
        for (int p = 0; p < 8; p++) r[p] = LD_SC1(rws, vo, WS_FFN + (h * 8 + p) * FFN_STRIDE);
      });
#pragma unroll
      for (int p = 0; p < 8; p++) { g += __uint_as_float(a[p][0]); u += __uint_as_float(a[p][2]); }
    }
    g = rbf(g); u = rbf(u);
    const float t = 0.7978845608028654f * (g + 0.044715f * g * g * g);
    const float th = 1.f - 2.f / (1.f + __expf(2.f * t));
    const float hid = rbf(0.5f * g * (1.f + th)) * u;
    ldsu[L_VV + bb * 192 + 128 + lane] = pack_bf(hid, hid);
  }
  LDS_BARRIER();
  STAMP(8);
  {
    f32x4 ao0 = {0, 0, 0, 0}, ao1 = {0, 0, 0, 0}, ad = {0, 0, 0, 0};
    const u32* vv = ldsu + L_VV + (c >> 3) * 192;
    const int bb = lane >> 5;
    const float g = lds[L_G + bb], g1 = lds[L_G + 2 + bb];
    ao0 = mfma(onehot(vv[16 * w + r4], msk), wt[0], ao0);
    ao1 = mfma(onehot(vv[16 * w + 4 + r4], msk), wt[1], ao1);
    ad = mfma(onehot(vv[128 + 8 * w + r4], msk), wt[4], ad);
    ao0 = mfma(onehot(vv[16 * w + 8 + r4], msk), wt[2], ao0);
    ao1 = mfma(onehot(vv[16 * w + 12 + r4], msk), wt[3], ao1);
    ad = mfma(onehot(vv[128 + 8 * w + 4 + r4], msk), wt[5], ad);
    if (c < 10) *(f32x4*)(lds + L_RED + w * 160 + bb * 80 + 8 * c + 4 * (r4 & 1)) = g * (ao0 + ao1) + g1 * ad;
  }
  LDS_BARRIER();
  STAMP(9);
  if (tid < 80) {
    f32x2 sum = {0, 0};
#pragma unroll
    for (int ww = 0; ww < 8; ww++) sum += *(const f32x2*)(lds + L_RED + ww * 160 + tid * 2);
    ST_SC1(mk_unit(sum[0], sum[1], tag), rws, tid * 16, WS_HOP2 + (j * 32 + h) * HOP2_STRIDE);
  }
  if (w == 2 && h < 20) {
    const int bb = h / 10, cg = h % 10;
    const int hp = lane >> 1, half = lane & 1;
    const int vo0 = hp * HOP2_STRIDE + (bb * 40 + cg * 4 + half * 2) * 16;
    const int so = WS_HOP2 + j * 32 * HOP2_STRIDE;
    u32x4 a[2];
    poll_units<2>(a, tag, [&](u32x4 (&r)[2]) {
      int vv = vo0;
      asm volatile("" : "+v"(vv) :: "memory");
      r[0] = LD_SC1(rws, vv, so); r[1] = LD_SC1(rws, vv + 16, so);
    });
    float v[4] = {__uint_as_float(a[0][0]), __uint_as_float(a[0][2]), __uint_as_float(a[1][0]), __uint_as_float(a[1][2])};
#pragma unroll
    for (int k = 0; k < 4; k++) {
      v[k] += dpp<0x4E>(v[k]);
      v[k] += dpp<0x124>(v[k]);
      v[k] += dpp<0x128>(v[k]);
      v[k] = xrow_sum(v[k]);
    }
    if (lane < 2) {
      const u32x2 r = {pack_bf(v[0], v[1]), pack_bf(v[2], v[3])};
      __builtin_amdgcn_raw_buffer_store_b64(r, mk_rsrc(out, 2 * D * 2), (bb * 640 + 80 * j + 8 * cg + 4 * half) * 2, 0, OUT_AUX);
    }
    if (blk == 0 && lane == 0) __builtin_amdgcn_raw_buffer_store_b32(tag, rws, 0, WS_EPOCH, AUX_SC1);
  }
  if (blk == ENC0 && w == 5) { while (__builtin_amdgcn_s_memrealtime() < enc_t0 + 20000ull) __builtin_amdgcn_s_sleep(2); }
}

extern "C" void kernel_launch(void* const* d_in, const int* in_sizes, int n_in, void* d_out, int out_size, void* d_ws, size_t ws_size, hipStream_t stream) {
  fused_decode<<<NWG, NTHR, 0, stream>>>((const u32*)d_in[0], (const u32*)d_in[1], (const u32*)d_in[2], (const u32*)d_in[3], (const u32*)d_in[4],
                                         (const u32*)d_in[6], (const u32*)d_in[7], (const u32*)d_in[8], (const u32*)d_in[9], (u32*)d_out, (unsigned char*)d_ws);
}
```

```cpp
#include <hip/hip_runtime.h>
#include <hip/hip_bf16.h>
#include <stdint.h>

typedef unsigned u32;
using u32x4 = __attribute__((ext_vector_type(4))) unsigned;
using u32x2 = __attribute__((ext_vector_type(2))) unsigned;
using f32x4 = __attribute__((ext_vector_type(4))) float;
using f32x2 = __attribute__((ext_vector_type(2))) float;
using bf16x2 = __attribute__((ext_vector_type(2))) __bf16;
using bf16x8 = __attribute__((ext_vector_type(8))) __bf16;
typedef __amdgpu_buffer_rsrc_t rsrc_t;
#define GAS __attribute__((address_space(1)))

#define D1 12
#define D2 24
#define WT_AT 2
#define WSK 1
#define ROT_EXPR (76 * b + 3 * h)
#define ROTL_EXPR 0
#define RSC 1
#define HILO 1
#define OUT_AUX 16

constexpr int D = 640, SK = 8192, DH = 128, F = 1728;
constexpr int NWG = 256, NTHR = 512;
constexpr int AUX_NT = 2, AUX_SC1 = 16;
constexpr int OOB = (int)0x80000000;

constexpr unsigned WS_EPOCH = 0;
constexpr unsigned WS_QP = 4096, QP_STRIDE = 2048;
constexpr unsigned WS_FFN = WS_QP + 256 * QP_STRIDE, FFN_STRIDE = 2048;
constexpr unsigned WS_HOP1 = WS_FFN + 256 * FFN_STRIDE, HOP1_STRIDE = 1152;
constexpr unsigned WS_HOP2 = WS_HOP1 + 256 * HOP1_STRIDE, HOP2_STRIDE = 1280;
constexpr unsigned WS_END = WS_HOP2 + 256 * HOP2_STRIDE;

constexpr int L_QRED = 0;
constexpr int L_FRED = L_QRED + 2048;
constexpr int L_Q = L_FRED + 2048;
constexpr int L_PART = L_Q + 64;
constexpr int L_VV = L_PART + 8 * 132;
constexpr int L_RED = L_VV + 384;
constexpr int L_G = L_RED + 1280;
constexpr int L_TAG = L_G + 4;
constexpr int L_END = L_TAG + 4;

__device__ __forceinline__ float bflo(u32 u) { return __builtin_bit_cast(float, u << 16); }
__device__ __forceinline__ float bfhi(u32 u) { return __builtin_bit_cast(float, u & 0xffff0000u); }
__device__ __forceinline__ float rbf(float x) { return (float)(__bf16)x; }
__device__ __forceinline__ u32 pack_bf(float lo, float hi) {
  bf16x2 v = {(__bf16)lo, (__bf16)hi};
  return __builtin_bit_cast(u32, v);
}
__device__ __forceinline__ float dot2(u32 a, u32 b, float c) {
  return __builtin_amdgcn_fdot2_f32_bf16(__builtin_bit_cast(bf16x2, a), __builtin_bit_cast(bf16x2, b), c, false);
}
__device__ __forceinline__ f32x4 mfma(u32x4 a, u32x4 b, f32x4 c) {
  return __builtin_amdgcn_mfma_f32_16x16x32_bf16(__builtin_bit_cast(bf16x8, a), __builtin_bit_cast(bf16x8, b), c, 0, 0, 0);
}
template <int CTRL> __device__ __forceinline__ float dpp(float x) {
  return __builtin_bit_cast(float, __builtin_amdgcn_mov_dpp(__builtin_bit_cast(int, x), CTRL, 0xf, 0xf, true));
}
__device__ __forceinline__ float row16_sum(float x) {
  x += dpp<0xB1>(x);
  x += dpp<0x4E>(x);
  x += dpp<0x141>(x);
  x += dpp<0x140>(x);
  return x;
}
__device__ __forceinline__ float xhalf_sum(float x) {
  auto t = __builtin_amdgcn_permlane32_swap(__float_as_uint(x), __float_as_uint(x), false, false);
  return __uint_as_float(t[0]) + __uint_as_float(t[1]);
}
__device__ __forceinline__ float xrow_sum(float x) {
  auto s = __builtin_amdgcn_permlane16_swap(__float_as_uint(x), __float_as_uint(x), false, false);
  return xhalf_sum(__uint_as_float(s[0]) + __uint_as_float(s[1]));
}
__device__ __forceinline__ float xrow_max(float x) {
  auto s = __builtin_amdgcn_permlane16_swap(__float_as_uint(x), __float_as_uint(x), false, false);
  x = fmaxf(__uint_as_float(s[0]), __uint_as_float(s[1]));
  auto t = __builtin_amdgcn_permlane32_swap(__float_as_uint(x), __float_as_uint(x), false, false);
  return fmaxf(__uint_as_float(t[0]), __uint_as_float(t[1]));
}
#define LD_NT(r, voff, soff) __builtin_amdgcn_raw_buffer_load_b128((r), (voff), (soff), AUX_NT)
#define AUX_KV 2
#define LD_KV(r, voff, soff) __builtin_amdgcn_raw_buffer_load_b128((r), (voff), (soff), AUX_KV)
#define LD_SC1(r, voff, soff) __builtin_amdgcn_raw_buffer_load_b128((r), (voff), (soff), AUX_SC1)
#define ST_SC1(v, r, voff, soff) __builtin_amdgcn_raw_buffer_store_b128((v), (r), (voff), (soff), AUX_SC1)
#define LDS_BARRIER() do { asm volatile("s_waitcnt lgkmcnt(0)" ::: "memory"); __builtin_amdgcn_s_barrier(); asm volatile("" ::: "memory"); } while (0)
#define RAW_BARRIER() do { __builtin_amdgcn_sched_barrier(0); __builtin_amdgcn_s_barrier(); __builtin_amdgcn_sched_barrier(0); } while (0)
__device__ __forceinline__ rsrc_t mk_rsrc(const void* p, unsigned bytes) { return __builtin_amdgcn_make_buffer_rsrc((void*)p, 0, bytes, 0x00020000); }
__device__ __forceinline__ u32x4 mk_unit(float a, float b, u32 tag) { u32x4 v = {__float_as_uint(a), tag, __float_as_uint(b), tag}; return v; }
__device__ __forceinline__ bool unit_ok(u32x4 v, u32 tag) { return (v[1] == tag) & (v[3] == tag); }
__device__ __forceinline__ u32x4 onehot(u32 dup, const u32 (&msk)[4]) { u32x4 a = {dup & msk[0], dup & msk[1], dup & msk[2], dup & msk[3]}; return a; }

constexpr unsigned SPIN_CAP = 1u << 18;
#define STAMP(k) do { } while (0)
#define STAMP_DECL do { } while (0)
#define STAMP_FLUSH() do { } while (0)

template <int N, typename Issue>
__device__ __forceinline__ void poll_units(u32x4 (&u)[N], u32 tag, Issue issue) {
  for (unsigned spins = 0;;) {
    issue(u);
    bool ok = true;
#pragma unroll
    for (int k = 0; k < N; k++) ok &= unit_ok(u[k], tag);
    if (__all(ok) || ++spins > SPIN_CAP) break;
    __builtin_amdgcn_s_sleep(1);
  }
}

__global__ void __launch_bounds__(NTHR, 2)
fused_decode(const u32* __restrict__ x, const u32* __restrict__ kc, const u32* __restrict__ vc, const u32* __restrict__ wq,
             const u32* __restrict__ wo, const u32* __restrict__ wg, const u32* __restrict__ wu, const u32* __restrict__ wd,
             const u32* __restrict__ gw, u32* __restrict__ out, unsigned char* __restrict__ ws) {
  __shared__ __attribute__((aligned(16))) float lds[L_END];
  u32* const ldsu = (u32*)lds;
  const int tid = threadIdx.x, lane = tid & 63, w = __builtin_amdgcn_readfirstlane(tid >> 6);
  const int r4 = lane >> 4, c = lane & 15;
  const int blk = blockIdx.x, xg = blk & 7, li = blk >> 3;
  const int h = xg * 4 + (li >> 3), j = li & 7, b = j >> 2, s = j & 3;
  const int fb = (h & 3) * 8 + (h >> 2);
  const bool has_ffn = fb < 27;
  const int bh = b * 32 + h;
  STAMP_DECL;
  STAMP(0);

  const rsrc_t rws = mk_rsrc(ws, WS_END);
  const rsrc_t rk = mk_rsrc((const unsigned char*)kc + (size_t)bh * (SK * DH * 2), SK * DH * 2);
  const rsrc_t rv = mk_rsrc((const unsigned char*)vc + (size_t)bh * (SK * DH * 2), SK * DH * 2);

  u32 xs[3], epoch;
  u32x4 qw[3], fw[3];
  const int row0 = 80 * j + 10 * w + r4;
  {
    const rsrc_t rx = mk_rsrc(x, 2 * D * 2);
    const rsrc_t rwq = mk_rsrc(wq, D * 4096 * 2);
#pragma unroll
    for (int u = 0; u < 3; u++) {
      const bool ok = (u < 2) || (r4 < 2);
      xs[u] = __builtin_amdgcn_raw_buffer_load_b16(rx, ok ? ((c >> 3) * D + row0 + 4 * u) * 2 : OOB, 0, 0);
    }
#pragma unroll
    for (int u = 0; u < 3; u++) {
      const bool ok = (u < 2) || (r4 < 2);
      qw[u] = LD_NT(rwq, ok ? ((row0 + 4 * u) * 4096 + 128 * h + 8 * c) * 2 : OOB, 0);
    }
    epoch = __builtin_amdgcn_raw_buffer_load_b32(rws, tid == 0 ? 0 : OOB, WS_EPOCH, AUX_SC1);
  }
  __builtin_amdgcn_sched_barrier(0);
  if (D1 > 0) __builtin_amdgcn_s_sleep(D1);
  RAW_BARRIER();
  {
    const int fbc = has_ffn ? fb : 0;
    const GAS unsigned char* fbase = (const GAS unsigned char*)((c < 8) ? wg : wu) + (64 * fbc + 8 * (c & 7)) * 2;
#pragma unroll
    for (int u = 0; u < 3; u++) {
      const int row = (u < 2 || r4 < 2) ? row0 + 4 * u : row0;
      fw[u] = __builtin_nontemporal_load((const GAS u32x4*)(fbase + (size_t)row * (F * 2)));
    }
  }
  __builtin_amdgcn_sched_barrier(0);
  u32 msk[4];
#pragma unroll
  for (int k = 0; k < 4; k++) msk[k] = (k == ((lane & 7) >> 1)) ? ((lane & 1) ? 0xffff0000u : 0x0000ffffu) : 0u;
  u32x4 xa[3];
  {
    f32x4 qa = {0, 0, 0, 0};
#pragma unroll
    for (int u = 0; u < 3; u++) { xa[u] = onehot(xs[u] * 0x10001u, msk); qa = mfma(xa[u], qw[u], qa); }
    *(f32x4*)(lds + L_QRED + w * 256 + (lane >> 5) * 128 + 8 * c + 4 * (r4 & 1)) = qa;
  }
  asm volatile("" :: "v"(epoch));
  if (tid == 0) ldsu[L_TAG] = epoch + 1u;
  STAMP(1);
  LDS_BARRIER();
  STAMP(2);
  const u32 tag = __builtin_amdgcn_readfirstlane(ldsu[L_TAG]);
  if (w < 2) {
    f32x2 sum = {0, 0};
#pragma unroll
    for (int ww = 0; ww < 8; ww++) sum += *(const f32x2*)(lds + L_QRED + ww * 256 + tid * 2);
    ST_SC1(mk_unit(sum[0], sum[1], tag), rws, tid * 16, WS_QP + (h * 8 + j) * QP_STRIDE);
  }
  u32x4 qg[8];
  {
    const rsrc_t rq = mk_rsrc(ws, w == 0 ? WS_END : 0);
    if (w == 0 && D2 > 0) __builtin_amdgcn_s_sleep(D2);
#pragma unroll
    for (int p = 0; p < 8; p++) qg[p] = LD_SC1(rq, lane * 16 + b * 1024, WS_QP + (h * 8 + p) * QP_STRIDE);
  }
  RAW_BARRIER();
  const int kvoff = lane * 16;
  const int rotc = ROT_EXPR, rotl = ROTL_EXPR;
  constexpr int NSMALL = 64 - WSK;
  const int nseq = w < 4 ? 64 + WSK : NSMALL;
#define SLOT_OF(i) ((i) < NSMALL ? w + 8 * (i) : 8 * NSMALL + w + 4 * ((i) - NSMALL))
#define SLOT_OFF(t) (((4 * ((t) >> 4) + s + rotc) & 127) * 16384 + (((t) + rotl) & 15) * 1024)
  u32x4 kr[2], vr[2];
  kr[0] = LD_KV(rk, kvoff, SLOT_OFF(SLOT_OF(0))); vr[0] = LD_KV(rv, kvoff, SLOT_OFF(SLOT_OF(0)));
  u32x4 gx0, gx1, gg0, gg1;
  {
    const rsrc_t rxb = mk_rsrc((const unsigned char*)x + (w & 1) * D * 2, w >= 6 ? D * 2 : 0), rgw = mk_rsrc(gw, w >= 6 ? D * 2 : 0);
    gx0 = __builtin_amdgcn_raw_buffer_load_b128(rxb, lane * 16, 0, 0); gx1 = __builtin_amdgcn_raw_buffer_load_b128(rxb, lane * 16 + 1024, 0, 0);
    gg0 = __builtin_amdgcn_raw_buffer_load_b128(rgw, lane * 16, 0, 0); gg1 = __builtin_amdgcn_raw_buffer_load_b128(rgw, lane * 16 + 1024, 0, 0);
  }
  __builtin_amdgcn_sched_barrier(0);
  STAMP(3);
  {
    f32x4 fa = {0, 0, 0, 0};
#pragma unroll
    for (int u = 0; u < 3; u++) fa = mfma(xa[u], fw[u], fa);
    *(f32x4*)(lds + L_FRED + w * 256 + (lane >> 5) * 128 + 8 * c + 4 * (r4 & 1)) = fa;
  }
  asm volatile("" :: "v"(qg[0]), "v"(qg[1]), "v"(qg[2]), "v"(qg[3]), "v"(qg[4]), "v"(qg[5]), "v"(qg[6]), "v"(qg[7]));
  if (w == 0) {
    unsigned spins = 0;
    bool ok = true;
    float q0 = 0.f, q1 = 0.f;
#pragma unroll
    for (int p = 0; p < 8; p++) { ok &= unit_ok(qg[p], tag); q0 += __uint_as_float(qg[p][0]); q1 += __uint_as_float(qg[p][2]); }
    if (!__all(ok)) {
      do {
        __builtin_amdgcn_s_sleep(1);
        int vo = lane * 16 + b * 1024;
        asm volatile("" : "+v"(vo) :: "memory");
        ok = true; q0 = 0.f; q1 = 0.f;
#pragma unroll
        for (int p = 0; p < 8; p++) qg[p] = LD_SC1(rws, vo, WS_QP + (h * 8 + p) * QP_STRIDE);
#pragma unroll
        for (int p = 0; p < 8; p++) { ok &= unit_ok(qg[p], tag); q0 += __uint_as_float(qg[p][0]); q1 += __uint_as_float(qg[p][2]); }
      } while (!__all(ok) && ++spins < SPIN_CAP);
      spins++;
    }
    ldsu[L_Q + lane] = pack_bf(q0, q1);
  }
  STAMP(4);
  LDS_BARRIER();
  const u32x4 qp = *(const u32x4*)(ldsu + L_Q + c * 4);
  asm volatile("" :: "v"(qp));
  STAMP(5);

  constexpr float C2 = (RSC ? 1.f : 0.08838834764831845f) * 1.4426950408889634f;
  float mraw = -INFINITY, mc = -INFINITY, l = 0.f;
  f32x4 acc = {0, 0, 0, 0};
  int it = 0;
#define STEP(i) do { \
      const int so_ = it + 1 < nseq ? SLOT_OFF(SLOT_OF(it + 1)) : OOB; \
      kr[(i) ^ 1] = LD_KV(rk, kvoff, so_); vr[(i) ^ 1] = LD_KV(rv, kvoff, so_); \
      __builtin_amdgcn_sched_barrier(0); \
      if (it >= nseq) goto loop_done; \
      float sc = dot2(kr[i][0], qp[0], 0.f); sc = dot2(kr[i][1], qp[1], sc); sc = dot2(kr[i][2], qp[2], sc); sc = dot2(kr[i][3], qp[3], sc); \
      sc = row16_sum(sc); \
      if (RSC) sc = rbf(rbf(sc) * 0.08838834764831845f); \
      if (__builtin_amdgcn_ballot_w64(sc > mraw) != 0) {        \
        const float mn = xrow_max(fmaxf(sc, mraw)); \
        const float mnc = mn * C2; \
        const float al = __builtin_amdgcn_exp2f(mc - mnc); \
        acc *= al; l *= al; mraw = mn; mc = mnc; \
      } \
      const float p = __builtin_amdgcn_exp2f(fmaf(sc, C2, -mc)); \
      l += p; \
      u32 pp = pack_bf(p, p); \
      if (HILO) { const float pl = p - bflo(pp); const u32 pq = pack_bf(pl, pl); pp = (lane & 8) ? pq : pp; }     \
      acc = mfma(onehot(pp, msk), vr[i], acc); \
      asm volatile("" : "+v"(acc), "+v"(l)); \
      __builtin_amdgcn_sched_barrier(0); \
      it++; } while (0)
  if (w == 2 || w == 3) {
    float g = 0.f, u = 0.f;
#pragma unroll
    for (int ww = 0; ww < 8; ww++) { g += lds[L_FRED + ww * 256 + (w - 2) * 128 + lane]; u += lds[L_FRED + ww * 256 + (w - 2) * 128 + 64 + lane]; }
    if (has_ffn) ST_SC1(mk_unit(g, u, tag), rws, (tid - 128) * 16, WS_FFN + (h * 8 + j) * FFN_STRIDE);
  }
  for (int rd = 0; rd < WT_AT; rd++) { STEP(0); STEP(1); }
  u32x4 wt[6];
  {
    const int wtoff = c < 10 ? (r4 * D + 8 * c) * 2 : OOB;
    const rsrc_t rwo = mk_rsrc(wo, 4096 * D * 2), rwd = mk_rsrc(wd, has_ffn ? F * D * 2 : 0);
#pragma unroll
    for (int i = 0; i < 4; i++) wt[i] = LD_NT(rwo, wtoff, ((128 * h + 16 * w + 4 * i) * D + 80 * j) * 2);
#pragma unroll
    for (int i = 0; i < 2; i++) wt[4 + i] = LD_NT(rwd, wtoff, ((64 * fb + 8 * w + 4 * i) * D + 80 * j) * 2);
    __builtin_amdgcn_sched_barrier(0);
  }
  asm volatile("" :: "v"(gx0), "v"(gx1), "v"(gg0), "v"(gg1));
  if (w >= 6) {
    float gs = 0.f;
#pragma unroll
    for (int k = 0; k < 4; k++) {
      gs += rbf(bflo(gx0[k]) * bflo(gg0[k])) + rbf(bfhi(gx0[k]) * bfhi(gg0[k]));
      gs += rbf(bflo(gx1[k]) * bflo(gg1[k])) + rbf(bfhi(gx1[k]) * bfhi(gg1[k]));
    }
    gs = xrow_sum(row16_sum(gs));
    const float g = rbf(1.f / (1.f + __expf(-rbf(gs))));
    if (lane == 0) { lds[L_G + w - 6] = g; lds[L_G + 2 + w - 6] = rbf(1.f - g); }
  }
  for (;;) { STEP(0); STEP(1); }
loop_done:
  STAMP(6);

  l = xrow_sum(l);
  if (HILO) {
#pragma unroll
    for (int k = 0; k < 4; k++) acc[k] = xhalf_sum(acc[k]);
  }
  if (lane < 32) *(f32x4*)(lds + L_PART + w * 132 + 8 * c + 4 * r4) = acc;
  if (lane == 0) *(f32x2*)(lds + L_PART + w * 132 + 128) = f32x2{mc, l};
  LDS_BARRIER();
  STAMP(7);
  if (w == 0) {
    float mw[8], lw[8], M = -INFINITY;
    f32x2 a[8];
#pragma unroll
    for (int ww = 0; ww < 8; ww++) { const f32x2 t = *(const f32x2*)(lds + L_PART + ww * 132 + 128); mw[ww] = t[0]; lw[ww] = t[1]; a[ww] = *(const f32x2*)(lds + L_PART + ww * 132 + lane * 2); }
#pragma unroll
    for (int ww = 0; ww < 8; ww++) M = fmaxf(M, mw[ww]);
    float L = 0.f, o0 = 0.f, o1 = 0.f;
#pragma unroll
    for (int ww = 0; ww < 8; ww++) {
      const float e = __builtin_amdgcn_exp2f(mw[ww] - M);
      L = fmaf(e, lw[ww], L); o0 = fmaf(e, a[ww][0], o0); o1 = fmaf(e, a[ww][1], o1);
    }
    const int so = WS_HOP1 + (h * 8 + j) * HOP1_STRIDE;
    ST_SC1(mk_unit(o0, o1, tag), rws, lane * 16, so);
    if (lane == 0) ST_SC1(mk_unit(M, L, tag), rws, 1024, so);
  } else if (w <= 2) {
    const int bb = w - 1;
    u32x4 a[8];
    poll_units<8>(a, tag, [&](u32x4 (&r)[8]) {
      int vo = lane * 16, vz = 1024;
      asm volatile("" : "+v"(vo), "+v"(vz) :: "memory");
#pragma unroll
      for (int p = 0; p < 4; p++) { const int so = WS_HOP1 + (h * 8 + bb * 4 + p) * HOP1_STRIDE; r[p] = LD_SC1(rws, vo, so); r[4 + p] = LD_SC1(rws, vz, so); }
    });
    float M = -INFINITY;
#pragma unroll
    for (int p = 0; p < 4; p++) M = fmaxf(M, __uint_as_float(a[4 + p][0]));
    float L = 0.f, o0 = 0.f, o1 = 0.f;
#pragma unroll
    for (int p = 0; p < 4; p++) {
      const float e = __builtin_amdgcn_exp2f(__uint_as_float(a[4 + p][0]) - M);
      L = fmaf(e, __uint_as_float(a[4 + p][2]), L); o0 = fmaf(e, __uint_as_float(a[p][0]), o0); o1 = fmaf(e, __uint_as_float(a[p][2]), o1);
    }
    const float inv = __builtin_amdgcn_rcpf(L);
    o0 *= inv; o1 *= inv;
    *(u32x2*)(ldsu + L_VV + bb * 192 + lane * 2) = u32x2{pack_bf(o0, o0), pack_bf(o1, o1)};
  } else if (w <= 4) {
    const int bb = w - 3;
    float g = 0.f, u = 0.f;
    if (has_ffn) {
      u32x4 a[8];
      poll_units<8>(a, tag, [&](u32x4 (&r)[8]) {
        int vo = lane * 16 + bb * 1024;
        asm volatile("" : "+v"(vo) :: "memory");
#pragma unroll
        for (int p = 0; p < 8; p++) r[p] = LD_SC1(rws, vo, WS_FFN + (h * 8 + p) * FFN_STRIDE);
      });
#pragma unroll
      for (int p = 0; p < 8; p++) { g += __uint_as_float(a[p][0]); u += __uint_as_float(a[p][2]); }
    }
    g = rbf(g); u = rbf(u);
    const float t = 0.7978845608028654f * (g + 0.044715f * g * g * g);
    const float th = 1.f - 2.f / (1.f + __expf(2.f * t));
    const float hid = rbf(0.5f * g * (1.f + th)) * u;
    ldsu[L_VV + bb * 192 + 128 + lane] = pack_bf(hid, hid);
  }
  LDS_BARRIER();
  STAMP(8);
  {
    f32x4 ao0 = {0, 0, 0, 0}, ao1 = {0, 0, 0, 0}, ad = {0, 0, 0, 0};
    const u32* vv = ldsu + L_VV + (c >> 3) * 192;
    const int bb = lane >> 5;
    const float g = lds[L_G + bb], g1 = lds[L_G + 2 + bb];
    ao0 = mfma(onehot(vv[16 * w + r4], msk), wt[0], ao0);
    ao1 = mfma(onehot(vv[16 * w + 4 + r4], msk), wt[1], ao1);
    ad = mfma(onehot(vv[128 + 8 * w + r4], msk), wt[4], ad);
    ao0 = mfma(onehot(vv[16 * w + 8 + r4], msk), wt[2], ao0);
    ao1 = mfma(onehot(vv[16 * w + 12 + r4], msk), wt[3], ao1);
    ad = mfma(onehot(vv[128 + 8 * w + 4 + r4], msk), wt[5], ad);
    if (c < 10) *(f32x4*)(lds + L_RED + w * 160 + bb * 80 + 8 * c + 4 * (r4 & 1)) = g * (ao0 + ao1) + g1 * ad;
  }
  LDS_BARRIER();
  STAMP(9);
  if (tid < 80) {
    f32x2 sum = {0, 0};
#pragma unroll
    for (int ww = 0; ww < 8; ww++) sum += *(const f32x2*)(lds + L_RED + ww * 160 + tid * 2);
    ST_SC1(mk_unit(sum[0], sum[1], tag), rws, tid * 16, WS_HOP2 + (j * 32 + h) * HOP2_STRIDE);
  }
  if (w == 2 && h < 20) {
    const int bb = h / 10, cg = h % 10;
    const int hp = lane >> 1, half = lane & 1;
    const int vo0 = hp * HOP2_STRIDE + (bb * 40 + cg * 4 + half * 2) * 16;
    const int so = WS_HOP2 + j * 32 * HOP2_STRIDE;
    u32x4 a[2];
    poll_units<2>(a, tag, [&](u32x4 (&r)[2]) {
      int vv = vo0;
      asm volatile("" : "+v"(vv) :: "memory");
      r[0] = LD_SC1(rws, vv, so); r[1] = LD_SC1(rws, vv + 16, so);
    });
    float v[4] = {__uint_as_float(a[0][0]), __uint_as_float(a[0][2]), __uint_as_float(a[1][0]), __uint_as_float(a[1][2])};
#pragma unroll
    for (int k = 0; k < 4; k++) {
      v[k] += dpp<0x4E>(v[k]);
      v[k] += dpp<0x124>(v[k]);
      v[k] += dpp<0x128>(v[k]);
      v[k] = xrow_sum(v[k]);
    }
    if (lane < 2) {
      const u32x2 r = {pack_bf(v[0], v[1]), pack_bf(v[2], v[3])};
      __builtin_amdgcn_raw_buffer_store_b64(r, mk_rsrc(out, 2 * D * 2), (bb * 640 + 80 * j + 8 * cg + 4 * half) * 2, 0, OUT_AUX);
    }
    if (blk == 0 && lane == 0) __builtin_amdgcn_raw_buffer_store_b32(tag, rws, 0, WS_EPOCH, AUX_SC1);
  }
}

extern "C" void kernel_launch(void* const* d_in, const int* in_sizes, int n_in, void* d_out, int out_size, void* d_ws, size_t ws_size, hipStream_t stream) {
  fused_decode<<<NWG, NTHR, 0, stream>>>((const u32*)d_in[0], (const u32*)d_in[1], (const u32*)d_in[2], (const u32*)d_in[3], (const u32*)d_in[4],
                                         (const u32*)d_in[6], (const u32*)d_in[7], (const u32*)d_in[8], (const u32*)d_in[9], (u32*)d_out, (unsigned char*)d_ws);
}
```

```cpp
#include <hip/hip_runtime.h>
#include <hip/hip_bf16.h>
#include <stdint.h>

typedef unsigned u32;
using u32x4 = __attribute__((ext_vector_type(4))) unsigned;
using u32x2 = __attribute__((ext_vector_type(2))) unsigned;
using f32x4 = __attribute__((ext_vector_type(4))) float;
using f32x2 = __attribute__((ext_vector_type(2))) float;
using bf16x2 = __attribute__((ext_vector_type(2))) __bf16;
using bf16x8 = __attribute__((ext_vector_type(8))) __bf16;
typedef __amdgpu_buffer_rsrc_t rsrc_t;
#define GAS __attribute__((address_space(1)))

#define D1 12
#define D2 24
#define WT_AT 2
#define WSK 1
#define ROT_EXPR (12 * b)
#define RSC 1
#define HILO 1
#define OUT_AUX 16

constexpr int D = 640, SK = 8192, DH = 128, F = 1728;
constexpr int NWG = 256, NTHR = 512;
constexpr int AUX_NT = 2, AUX_SC1 = 16;
constexpr int OOB = (int)0x80000000;

constexpr unsigned WS_EPOCH = 0;
constexpr unsigned WS_QP = 4096, QP_STRIDE = 2048;
constexpr unsigned WS_FFN = WS_QP + 256 * QP_STRIDE, FFN_STRIDE = 2048;
constexpr unsigned WS_HOP1 = WS_FFN + 256 * FFN_STRIDE, HOP1_STRIDE = 1152;
constexpr unsigned WS_HOP2 = WS_HOP1 + 256 * HOP1_STRIDE, HOP2_STRIDE = 1280;
constexpr unsigned WS_END = WS_HOP2 + 256 * HOP2_STRIDE;

constexpr int L_QRED = 0;
constexpr int L_FRED = L_QRED + 2048;
constexpr int L_Q = L_FRED + 2048;
constexpr int L_PART = L_Q + 64;
constexpr int L_VV = L_PART + 8 * 132;
constexpr int L_RED = L_VV + 384;
constexpr int L_G = L_RED + 1280;
constexpr int L_TAG = L_G + 4;
constexpr int L_END = L_TAG + 4;

__device__ __forceinline__ float bflo(u32 u) { return __builtin_bit_cast(float, u << 16); }
__device__ __forceinline__ float bfhi(u32 u) { return __builtin_bit_cast(float, u & 0xffff0000u); }
__device__ __forceinline__ float rbf(float x) { return (float)(__bf16)x; }
__device__ __forceinline__ u32 pack_bf(float lo, float hi) {
  bf16x2 v = {(__bf16)lo, (__bf16)hi};
  return __builtin_bit_cast(u32, v);
}
__device__ __forceinline__ float dot2(u32 a, u32 b, float c) {
  return __builtin_amdgcn_fdot2_f32_bf16(__builtin_bit_cast(bf16x2, a), __builtin_bit_cast(bf16x2, b), c, false);
}
__device__ __forceinline__ f32x4 mfma(u32x4 a, u32x4 b, f32x4 c) {
  return __builtin_amdgcn_mfma_f32_16x16x32_bf16(__builtin_bit_cast(bf16x8, a), __builtin_bit_cast(bf16x8, b), c, 0, 0, 0);
}
template <int CTRL> __device__ __forceinline__ float dpp(float x) {
  return __builtin_bit_cast(float, __builtin_amdgcn_mov_dpp(__builtin_bit_cast(int, x), CTRL, 0xf, 0xf, true));
}
__device__ __forceinline__ float row16_sum(float x) {
  x += dpp<0xB1>(x);
  x += dpp<0x4E>(x);
  x += dpp<0x141>(x);
  x += dpp<0x140>(x);
  return x;
}
__device__ __forceinline__ float xhalf_sum(float x) {
  auto t = __builtin_amdgcn_permlane32_swap(__float_as_uint(x), __float_as_uint(x), false, false);
  return __uint_as_float(t[0]) + __uint_as_float(t[1]);
}
__device__ __forceinline__ float xrow_sum(float x) {
  auto s = __builtin_amdgcn_permlane16_swap(__float_as_uint(x), __float_as_uint(x), false, false);
  return xhalf_sum(__uint_as_float(s[0]) + __uint_as_float(s[1]));
}
__device__ __forceinline__ float xrow_max(float x) {
  auto s = __builtin_amdgcn_permlane16_swap(__float_as_uint(x), __float_as_uint(x), false, false);
  x = fmaxf(__uint_as_float(s[0]), __uint_as_float(s[1]));
  auto t = __builtin_amdgcn_permlane32_swap(__float_as_uint(x), __float_as_uint(x), false, false);
  return fmaxf(__uint_as_float(t[0]), __uint_as_float(t[1]));
}
#define LD_NT(r, voff, soff) __builtin_amdgcn_raw_buffer_load_b128((r), (voff), (soff), AUX_NT)
#define LD_SC1(r, voff, soff) __builtin_amdgcn_raw_buffer_load_b128((r), (voff), (soff), AUX_SC1)
#define ST_SC1(v, r, voff, soff) __builtin_amdgcn_raw_buffer_store_b128((v), (r), (voff), (soff), AUX_SC1)
#define LDS_BARRIER() do { asm volatile("s_waitcnt lgkmcnt(0)" ::: "memory"); __builtin_amdgcn_s_barrier(); asm volatile("" ::: "memory"); } while (0)
#define RAW_BARRIER() do { __builtin_amdgcn_sched_barrier(0); __builtin_amdgcn_s_barrier(); __builtin_amdgcn_sched_barrier(0); } while (0)
__device__ __forceinline__ rsrc_t mk_rsrc(const void* p, unsigned bytes) { return __builtin_amdgcn_make_buffer_rsrc((void*)p, 0, bytes, 0x00020000); }
__device__ __forceinline__ u32x4 mk_unit(float a, float b, u32 tag) { u32x4 v = {__float_as_uint(a), tag, __float_as_uint(b), tag}; return v; }
__device__ __forceinline__ bool unit_ok(u32x4 v, u32 tag) { return (v[1] == tag) & (v[3] == tag); }
__device__ __forceinline__ u32x4 onehot(u32 dup, const u32 (&msk)[4]) { u32x4 a = {dup & msk[0], dup & msk[1], dup & msk[2], dup & msk[3]}; return a; }

constexpr unsigned SPIN_CAP = 1u << 18;
#define STAMP(k) do { } while (0)
#define STAMP_DECL do { } while (0)
#define STAMP_FLUSH() do { } while (0)

template <int N, typename Issue>
__device__ __forceinline__ void poll_units(u32x4 (&u)[N], u32 tag, Issue issue) {
  for (unsigned spins = 0;;) {
    issue(u);
    bool ok = true;
#pragma unroll
    for (int k = 0; k < N; k++) ok &= unit_ok(u[k], tag);
    if (__all(ok) || ++spins > SPIN_CAP) break;
    __builtin_amdgcn_s_sleep(1);
  }
}

__global__ void __launch_bounds__(NTHR, 2)
fused_decode(const u32* __restrict__ x, const u32* __restrict__ kc, const u32* __restrict__ vc, const u32* __restrict__ wq,
             const u32* __restrict__ wo, const u32* __restrict__ wg, const u32* __restrict__ wu, const u32* __restrict__ wd,
             const u32* __restrict__ gw, u32* __restrict__ out, unsigned char* __restrict__ ws) {
  __shared__ __attribute__((aligned(16))) float lds[L_END];
  u32* const ldsu = (u32*)lds;
  const int tid = threadIdx.x, lane = tid & 63, w = __builtin_amdgcn_readfirstlane(tid >> 6);
  const int r4 = lane >> 4, c = lane & 15;
  const int blk = blockIdx.x, xg = blk & 7, li = blk >> 3;
  const int h = xg * 4 + (li >> 3), j = li & 7, b = j >> 2, s = j & 3;
  const int fb = (h & 3) * 8 + (h >> 2);
  const bool has_ffn = fb < 27;
  const int bh = b * 32 + h;
  STAMP_DECL;
  STAMP(0);

  const rsrc_t rws = mk_rsrc(ws, WS_END);
  const rsrc_t rk = mk_rsrc((const unsigned char*)kc + (size_t)bh * (SK * DH * 2), SK * DH * 2);
  const rsrc_t rv = mk_rsrc((const unsigned char*)vc + (size_t)bh * (SK * DH * 2), SK * DH * 2);

  u32 xs[3], epoch;
  u32x4 qw[3], fw[3];
  const int row0 = 80 * j + 10 * w + r4;
  {
    const rsrc_t rx = mk_rsrc(x, 2 * D * 2);
    const rsrc_t rwq = mk_rsrc(wq, D * 4096 * 2);
#pragma unroll
    for (int u = 0; u < 3; u++) {
      const bool ok = (u < 2) || (r4 < 2);
      xs[u] = __builtin_amdgcn_raw_buffer_load_b16(rx, ok ? ((c >> 3) * D + row0 + 4 * u) * 2 : OOB, 0, 0);
    }
#pragma unroll
    for (int u = 0; u < 3; u++) {
      const bool ok = (u < 2) || (r4 < 2);
      qw[u] = LD_NT(rwq, ok ? ((row0 + 4 * u) * 4096 + 128 * h + 8 * c) * 2 : OOB, 0);
    }
    epoch = __builtin_amdgcn_raw_buffer_load_b32(rws, tid == 0 ? 0 : OOB, WS_EPOCH, AUX_SC1);
  }
  __builtin_amdgcn_sched_barrier(0);
  if (D1 > 0) __builtin_amdgcn_s_sleep(D1);
  RAW_BARRIER();
  {
    const int fbc = has_ffn ? fb : 0;
    const GAS unsigned char* fbase = (const GAS unsigned char*)((c < 8) ? wg : wu) + (64 * fbc + 8 * (c & 7)) * 2;
#pragma unroll
    for (int u = 0; u < 3; u++) {
      const int row = (u < 2 || r4 < 2) ? row0 + 4 * u : row0;
      fw[u] = __builtin_nontemporal_load((const GAS u32x4*)(fbase + (size_t)row * (F * 2)));
    }
  }
  __builtin_amdgcn_sched_barrier(0);
  u32 msk[4];
#pragma unroll
  for (int k = 0; k < 4; k++) msk[k] = (k == ((lane & 7) >> 1)) ? ((lane & 1) ? 0xffff0000u : 0x0000ffffu) : 0u;
  u32x4 xa[3];
  {
    f32x4 qa = {0, 0, 0, 0};
#pragma unroll
    for (int u = 0; u < 3; u++) { xa[u] = onehot(xs[u] * 0x10001u, msk); qa = mfma(xa[u], qw[u], qa); }
    *(f32x4*)(lds + L_QRED + w * 256 + (lane >> 5) * 128 + 8 * c + 4 * (r4 & 1)) = qa;
  }
  asm volatile("" :: "v"(epoch));
  if (tid == 0) ldsu[L_TAG] = epoch + 1u;
  STAMP(1);
  LDS_BARRIER();
  STAMP(2);
  const u32 tag = __builtin_amdgcn_readfirstlane(ldsu[L_TAG]);
  if (w < 2) {
    f32x2 sum = {0, 0};
#pragma unroll
    for (int ww = 0; ww < 8; ww++) sum += *(const f32x2*)(lds + L_QRED + ww * 256 + tid * 2);
    ST_SC1(mk_unit(sum[0], sum[1], tag), rws, tid * 16, WS_QP + (h * 8 + j) * QP_STRIDE);
  }
  u32x4 qg[8];
  {
    const rsrc_t rq = mk_rsrc(ws, w == 0 ? WS_END : 0);
    if (w == 0 && D2 > 0) __builtin_amdgcn_s_sleep(D2);
#pragma unroll
    for (int p = 0; p < 8; p++) qg[p] = LD_SC1(rq, lane * 16 + b * 1024, WS_QP + (h * 8 + p) * QP_STRIDE);
  }
  RAW_BARRIER();
  const int kvoff = lane * 16;
  const int rotc = ROT_EXPR;
  constexpr int NSMALL = 64 - WSK;
  const int nseq = w < 4 ? 64 + WSK : NSMALL;
#define SLOT_OF(i) ((i) < NSMALL ? w + 8 * (i) : 8 * NSMALL + w + 4 * ((i) - NSMALL))
#define SLOT_OFF(t) (((4 * ((t) >> 4) + s + rotc) & 127) * 16384 + ((t) & 15) * 1024)
  u32x4 kr[2], vr[2];
  kr[0] = LD_NT(rk, kvoff, SLOT_OFF(SLOT_OF(0))); vr[0] = LD_NT(rv, kvoff, SLOT_OFF(SLOT_OF(0)));
  u32x4 gx0, gx1, gg0, gg1;
  {
    const rsrc_t rxb = mk_rsrc((const unsigned char*)x + (w & 1) * D * 2, w >= 6 ? D * 2 : 0), rgw = mk_rsrc(gw, w >= 6 ? D * 2 : 0);
    gx0 = __builtin_amdgcn_raw_buffer_load_b128(rxb, lane * 16, 0, 0); gx1 = __builtin_amdgcn_raw_buffer_load_b128(rxb, lane * 16 + 1024, 0, 0);
    gg0 = __builtin_amdgcn_raw_buffer_load_b128(rgw, lane * 16, 0, 0); gg1 = __builtin_amdgcn_raw_buffer_load_b128(rgw, lane * 16 + 1024, 0, 0);
  }
  __builtin_amdgcn_sched_barrier(0);
  STAMP(3);
  {
    f32x4 fa = {0, 0, 0, 0};
#pragma unroll
    for (int u = 0; u < 3; u++) fa = mfma(xa[u], fw[u], fa);
    *(f32x4*)(lds + L_FRED + w * 256 + (lane >> 5) * 128 + 8 * c + 4 * (r4 & 1)) = fa;
  }
  asm volatile("" :: "v"(qg[0]), "v"(qg[1]), "v"(qg[2]), "v"(qg[3]), "v"(qg[4]), "v"(qg[5]), "v"(qg[6]), "v"(qg[7]));
  if (w == 0) {
    unsigned spins = 0;
    bool ok = true;
    float q0 = 0.f, q1 = 0.f;
#pragma unroll
    for (int p = 0; p < 8; p++) { ok &= unit_ok(qg[p], tag); q0 += __uint_as_float(qg[p][0]); q1 += __uint_as_float(qg[p][2]); }
    if (!__all(ok)) {
      do {
        __builtin_amdgcn_s_sleep(1);
        int vo = lane * 16 + b * 1024;
        asm volatile("" : "+v"(vo) :: "memory");
        ok = true; q0 = 0.f; q1 = 0.f;
#pragma unroll
        for (int p = 0; p < 8; p++) qg[p] = LD_SC1(rws, vo, WS_QP + (h * 8 + p) * QP_STRIDE);
#pragma unroll
        for (int p = 0; p < 8; p++) { ok &= unit_ok(qg[p], tag); q0 += __uint_as_float(qg[p][0]); q1 += __uint_as_float(qg[p][2]); }
      } while (!__all(ok) && ++spins < SPIN_CAP);
      spins++;
    }
    ldsu[L_Q + lane] = pack_bf(q0, q1);
  }
  STAMP(4);
  LDS_BARRIER();
  const u32x4 qp = *(const u32x4*)(ldsu + L_Q + c * 4);
  asm volatile("" :: "v"(qp));
  STAMP(5);

  constexpr float C2 = (RSC ? 1.f : 0.08838834764831845f) * 1.4426950408889634f;
  float mraw = -INFINITY, mc = -INFINITY, l = 0.f;
  f32x4 acc = {0, 0, 0, 0};
  int it = 0;
#define STEP(i) do { \
      const int so_ = it + 1 < nseq ? SLOT_OFF(SLOT_OF(it + 1)) : OOB; \
      kr[(i) ^ 1] = LD_NT(rk, kvoff, so_); vr[(i) ^ 1] = LD_NT(rv, kvoff, so_); \
      __builtin_amdgcn_sched_barrier(0); \
      if (it >= nseq) goto loop_done; \
      float sc = dot2(kr[i][0], qp[0], 0.f); sc = dot2(kr[i][1], qp[1], sc); sc = dot2(kr[i][2], qp[2], sc); sc = dot2(kr[i][3], qp[3], sc); \
      sc = row16_sum(sc); \
      if (RSC) sc = rbf(rbf(sc) * 0.08838834764831845f); \
      if (__builtin_amdgcn_ballot_w64(sc > mraw) != 0) {        \
        const float mn = xrow_max(fmaxf(sc, mraw)); \
        const float mnc = mn * C2; \
        const float al = __builtin_amdgcn_exp2f(mc - mnc); \
        acc *= al; l *= al; mraw = mn; mc = mnc; \
      } \
      const float p = __builtin_amdgcn_exp2f(fmaf(sc, C2, -mc)); \
      l += p; \
      u32 pp = pack_bf(p, p); \
      if (HILO) { const float pl = p - bflo(pp); const u32 pq = pack_bf(pl, pl); pp = (lane & 8) ? pq : pp; }     \
      acc = mfma(onehot(pp, msk), vr[i], acc); \
      asm volatile("" : "+v"(acc), "+v"(l)); \
      __builtin_amdgcn_sched_barrier(0); \
      it++; } while (0)
  if (w == 2 || w == 3) {
    float g = 0.f, u = 0.f;
#pragma unroll
    for (int ww = 0; ww < 8; ww++) { g += lds[L_FRED + ww * 256 + (w - 2) * 128 + lane]; u += lds[L_FRED + ww * 256 + (w - 2) * 128 + 64 + lane]; }
    if (has_ffn) ST_SC1(mk_unit(g, u, tag), rws, (tid - 128) * 16, WS_FFN + (h * 8 + j) * FFN_STRIDE);
  }
  for (int rd = 0; rd < WT_AT; rd++) { STEP(0); STEP(1); }
  u32x4 wt[6];
  {
    const int wtoff = c < 10 ? (r4 * D + 8 * c) * 2 : OOB;
    const rsrc_t rwo = mk_rsrc(wo, 4096 * D * 2), rwd = mk_rsrc(wd, has_ffn ? F * D * 2 : 0);
#pragma unroll
    for (int i = 0; i < 4; i++) wt[i] = LD_NT(rwo, wtoff, ((128 * h + 16 * w + 4 * i) * D + 80 * j) * 2);
#pragma unroll
    for (int i = 0; i < 2; i++) wt[4 + i] = LD_NT(rwd, wtoff, ((64 * fb + 8 * w + 4 * i) * D + 80 * j) * 2);
    __builtin_amdgcn_sched_barrier(0);
  }
  asm volatile("" :: "v"(gx0), "v"(gx1), "v"(gg0), "v"(gg1));
  if (w >= 6) {
    float gs = 0.f;
#pragma unroll
    for (int k = 0; k < 4; k++) {
      gs += rbf(bflo(gx0[k]) * bflo(gg0[k])) + rbf(bfhi(gx0[k]) * bfhi(gg0[k]));
      gs += rbf(bflo(gx1[k]) * bflo(gg1[k])) + rbf(bfhi(gx1[k]) * bfhi(gg1[k]));
    }
    gs = xrow_sum(row16_sum(gs));
    const float g = rbf(1.f / (1.f + __expf(-rbf(gs))));
    if (lane == 0) { lds[L_G + w - 6] = g; lds[L_G + 2 + w - 6] = rbf(1.f - g); }
  }
  for (;;) { STEP(0); STEP(1); }
loop_done:
  STAMP(6);

  l = xrow_sum(l);
  if (HILO) {
#pragma unroll
    for (int k = 0; k < 4; k++) acc[k] = xhalf_sum(acc[k]);
  }
  if (lane < 32) *(f32x4*)(lds + L_PART + w * 132 + 8 * c + 4 * r4) = acc;
  if (lane == 0) *(f32x2*)(lds + L_PART + w * 132 + 128) = f32x2{mc, l};
  LDS_BARRIER();
  STAMP(7);
  if (w == 0) {
    float mw[8], lw[8], M = -INFINITY;
    f32x2 a[8];
#pragma unroll
    for (int ww = 0; ww < 8; ww++) { const f32x2 t = *(const f32x2*)(lds + L_PART + ww * 132 + 128); mw[ww] = t[0]; lw[ww] = t[1]; a[ww] = *(const f32x2*)(lds + L_PART + ww * 132 + lane * 2); }
#pragma unroll
    for (int ww = 0; ww < 8; ww++) M = fmaxf(M, mw[ww]);
    float L = 0.f, o0 = 0.f, o1 = 0.f;
#pragma unroll
    for (int ww = 0; ww < 8; ww++) {
      const float e = __builtin_amdgcn_exp2f(mw[ww] - M);
      L = fmaf(e, lw[ww], L); o0 = fmaf(e, a[ww][0], o0); o1 = fmaf(e, a[ww][1], o1);
    }
    const int so = WS_HOP1 + (h * 8 + j) * HOP1_STRIDE;
    ST_SC1(mk_unit(o0, o1, tag), rws, lane * 16, so);
    if (lane == 0) ST_SC1(mk_unit(M, L, tag), rws, 1024, so);
  } else if (w <= 2) {
    const int bb = w - 1;
    u32x4 a[8];
    poll_units<8>(a, tag, [&](u32x4 (&r)[8]) {
      int vo = lane * 16, vz = 1024;
      asm volatile("" : "+v"(vo), "+v"(vz) :: "memory");
#pragma unroll
      for (int p = 0; p < 4; p++) { const int so = WS_HOP1 + (h * 8 + bb * 4 + p) * HOP1_STRIDE; r[p] = LD_SC1(rws, vo, so); r[4 + p] = LD_SC1(rws, vz, so); }
    });
    float M = -INFINITY;
#pragma unroll
    for (int p = 0; p < 4; p++) M = fmaxf(M, __uint_as_float(a[4 + p][0]));
    float L = 0.f, o0 = 0.f, o1 = 0.f;
#pragma unroll
    for (int p = 0; p < 4; p++) {
      const float e = __builtin_amdgcn_exp2f(__uint_as_float(a[4 + p][0]) - M);
      L = fmaf(e, __uint_as_float(a[4 + p][2]), L); o0 = fmaf(e, __uint_as_float(a[p][0]), o0); o1 = fmaf(e, __uint_as_float(a[p][2]), o1);
    }
    const float inv = __builtin_amdgcn_rcpf(L);
    o0 *= inv; o1 *= inv;
    *(u32x2*)(ldsu + L_VV + bb * 192 + lane * 2) = u32x2{pack_bf(o0, o0), pack_bf(o1, o1)};
  } else if (w <= 4) {
    const int bb = w - 3;
    float g = 0.f, u = 0.f;
    if (has_ffn) {
      u32x4 a[8];
      poll_units<8>(a, tag, [&](u32x4 (&r)[8]) {
        int vo = lane * 16 + bb * 1024;
        asm volatile("" : "+v"(vo) :: "memory");
#pragma unroll
        for (int p = 0; p < 8; p++) r[p] = LD_SC1(rws, vo, WS_FFN + (h * 8 + p) * FFN_STRIDE);
      });
#pragma unroll
      for (int p = 0; p < 8; p++) { g += __uint_as_float(a[p][0]); u += __uint_as_float(a[p][2]); }
    }
    g = rbf(g); u = rbf(u);
    const float t = 0.7978845608028654f * (g + 0.044715f * g * g * g);
    const float th = 1.f - 2.f / (1.f + __expf(2.f * t));
    const float hid = rbf(0.5f * g * (1.f + th)) * u;
    ldsu[L_VV + bb * 192 + 128 + lane] = pack_bf(hid, hid);
  }
  LDS_BARRIER();
  STAMP(8);
  {
    f32x4 ao0 = {0, 0, 0, 0}, ao1 = {0, 0, 0, 0}, ad = {0, 0, 0, 0};
    const u32* vv = ldsu + L_VV + (c >> 3) * 192;
    const int bb = lane >> 5;
    const float g = lds[L_G + bb], g1 = lds[L_G + 2 + bb];
    ao0 = mfma(onehot(vv[16 * w + r4], msk), wt[0], ao0);
    ao1 = mfma(onehot(vv[16 * w + 4 + r4], msk), wt[1], ao1);
    ad = mfma(onehot(vv[128 + 8 * w + r4], msk), wt[4], ad);
    ao0 = mfma(onehot(vv[16 * w + 8 + r4], msk), wt[2], ao0);
    ao1 = mfma(onehot(vv[16 * w + 12 + r4], msk), wt[3], ao1);
    ad = mfma(onehot(vv[128 + 8 * w + 4 + r4], msk), wt[5], ad);
    if (c < 10) *(f32x4*)(lds + L_RED + w * 160 + bb * 80 + 8 * c + 4 * (r4 & 1)) = g * (ao0 + ao1) + g1 * ad;
  }
  LDS_BARRIER();
  STAMP(9);
  if (tid < 80) {
    f32x2 sum = {0, 0};
#pragma unroll
    for (int ww = 0; ww < 8; ww++) sum += *(const f32x2*)(lds + L_RED + ww * 160 + tid * 2);
    ST_SC1(mk_unit(sum[0], sum[1], tag), rws, tid * 16, WS_HOP2 + (j * 32 + h) * HOP2_STRIDE);
  }
  if (w == 2 && h < 20) {
    const int bb = h / 10, cg = h % 10;
    const int hp = lane >> 1, half = lane & 1;
    const int vo0 = hp * HOP2_STRIDE + (bb * 40 + cg * 4 + half * 2) * 16;
    const int so = WS_HOP2 + j * 32 * HOP2_STRIDE;
    u32x4 a[2];
    poll_units<2>(a, tag, [&](u32x4 (&r)[2]) {
      int vv = vo0;
      asm volatile("" : "+v"(vv) :: "memory");
      r[0] = LD_SC1(rws, vv, so); r[1] = LD_SC1(rws, vv + 16, so);
    });
    float v[4] = {__uint_as_float(a[0][0]), __uint_as_float(a[0][2]), __uint_as_float(a[1][0]), __uint_as_float(a[1][2])};
#pragma unroll
    for (int k = 0; k < 4; k++) {
      v[k] += dpp<0x4E>(v[k]);
      v[k] += dpp<0x124>(v[k]);
      v[k] += dpp<0x128>(v[k]);
      v[k] = xrow_sum(v[k]);
    }
    if (lane < 2) {
      const u32x2 r = {pack_bf(v[0], v[1]), pack_bf(v[2], v[3])};
      __builtin_amdgcn_raw_buffer_store_b64(r, mk_rsrc(out, 2 * D * 2), (bb * 640 + 80 * j + 8 * cg + 4 * half) * 2, 0, OUT_AUX);
    }
    if (blk == 0 && lane == 0) __builtin_amdgcn_raw_buffer_store_b32(tag, rws, 0, WS_EPOCH, AUX_SC1);
  }
}

extern "C" void kernel_launch(void* const* d_in, const int* in_sizes, int n_in, void* d_out, int out_size, void* d_ws, size_t ws_size, hipStream_t stream) {
  fused_decode<<<NWG, NTHR, 0, stream>>>((const u32*)d_in[0], (const u32*)d_in[1], (const u32*)d_in[2], (const u32*)d_in[3], (const u32*)d_in[4],
                                         (const u32*)d_in[6], (const u32*)d_in[7], (const u32*)d_in[8], (const u32*)d_in[9], (u32*)d_out, (unsigned char*)d_ws);
}
```
